```python
import torch

def _softmax(x, axis=-1):
    return torch.softmax(x, dim=axis)
def _gelu(x):
    return torch.nn.functional.gelu(x, approximate="tanh")
def _silu(x):
    return torch.nn.functional.silu(x)
def _sigmoid(x):
    return torch.sigmoid(x)
def _layernorm(x, axis=-1, eps=1e-5):
    m = x.mean(dim=axis, keepdim=True)
    v = x.var(dim=axis, keepdim=True, unbiased=False)
    return (x - m) / torch.sqrt(v + eps)
def _scatter_add(tgt, idx, src, axis=0):
    return tgt.clone().index_add_(axis, idx, src)
def _segment_sum(src, seg, n):
    out = torch.zeros((n,) + src.shape[1:], dtype=src.dtype)
    return out.index_add_(0, seg, src)
def _pad(x, pw):
    flat = tuple(v for pair in reversed(pw) for v in pair)
    return torch.nn.functional.pad(x, flat)

kernel_name = 'parallel_sum__gqa_block_cached__dense_activation_squared_relu__decode'

def setup_inputs():

    _w = (lambda a: (lambda t: t.to(torch.bfloat16) if t.is_floating_point() else t)(torch.as_tensor(a)))
    def _A_setup():
        B, Sq, D = 4, 1, 640
        Hq, Hkv, Sk, Dh = 32, 32, 8192, 192
        assert Hq % Hkv == 0, (Hq, Hkv)
        return {
            "x": _w((lambda *s: torch.randn(*s, dtype=torch.bfloat16) * 0.05)(B, Sq, D)),
            "k_cache": _w((lambda *s: torch.randn(*s, dtype=torch.bfloat16) * 1.6)(B, Hkv, Sk, Dh)),
            "v_cache": _w((lambda *s: torch.randn(*s, dtype=torch.bfloat16) * 3.2)(B, Hkv, Sk, Dh)),
            "wq": _w((lambda *s: torch.randn(*s, dtype=torch.bfloat16) * 1.6)(D, Hq * Dh) * (D ** -0.5)),
            "wo": _w((lambda *s: torch.randn(*s, dtype=torch.bfloat16) * 3.2)(Hq * Dh, D) * ((Hq * Dh) ** -0.5)),
        }
    _A_in = _A_setup()

    def _B_setup():
        B, S, D, F = 4, 1, 640, 1536
        return {
            "x": _w((lambda *s: torch.randn(*s, dtype=torch.bfloat16) * 0.05)(B, S, D)),
            "w1": _w((lambda *s: torch.randn(*s, dtype=torch.bfloat16) * 0.05)(D, F)),
            "b1": _w((lambda *s: torch.randn(*s, dtype=torch.bfloat16) * 0.2)(F)),
            "w2": _w((lambda *s: torch.randn(*s, dtype=torch.bfloat16) * 0.05)(F, D)),
            "b2": _w((lambda *s: torch.randn(*s, dtype=torch.bfloat16) * 0.8)(D)),
        }
    _B_in = _B_setup()

    return {
        "A_x": _A_in["x"],
        "A_k_cache": _A_in["k_cache"],
        "A_v_cache": _A_in["v_cache"],
        "A_wq": _A_in["wq"],
        "A_wo": _A_in["wo"],
        "B_x": _B_in["x"],
        "B_w1": _B_in["w1"],
        "B_b1": _B_in["b1"],
        "B_w2": _B_in["w2"],
        "B_b2": _B_in["b2"],
    }

def reference(A_x, A_k_cache, A_v_cache, A_wq, A_wo, B_x, B_w1, B_b1, B_w2, B_b2):
    def _A_ref(x, k_cache, v_cache, wq, wo):
        B, Sq, D = x.shape
        Hq, Dh = 32, 192
        Hkv = k_cache.shape[1]
        rep = Hq // Hkv
        q = (lambda x, *d: x.permute(*d))((x @ wq).reshape(B, Sq, Hq, Dh), 0, 2, 1, 3)
        k = (lambda x, n, axis: torch.repeat_interleave(x, n, dim=axis))(k_cache, rep, axis=1)
        v = (lambda x, n, axis: torch.repeat_interleave(x, n, dim=axis))(v_cache, rep, axis=1)
        scores = torch.einsum("bhqd,bhkd->bhqk", q, k) * (Dh ** -0.5)
        p = _softmax(scores, axis=-1)
        o = torch.einsum("bhqk,bhkd->bhqd", p, v)
        return (lambda x, *d: x.permute(*d))(o, 0, 2, 1, 3).reshape(B, Sq, Hq * Dh) @ wo

    def _B_ref(x, w1, b1, w2, b2):
        h = torch.einsum("bsd,df->bsf", x, w1) + b1
        zero = torch.zeros_like(h)
        r = torch.where(h > zero, h, zero)
        h = r * r
        out = torch.einsum("bsf,fd->bsd", h, w2) + b2
        return out

    x = A_x
    xn = _layernorm(x, axis=-1)
    return x + _A_ref(xn, A_k_cache, A_v_cache, A_wq, A_wo) + _B_ref(xn, B_w1, B_b1, B_w2, B_b2)
```

```cpp
#include <hip/hip_runtime.h>
#include <hip/hip_bf16.h>
#include <stdint.h>

typedef unsigned v4u __attribute__((ext_vector_type(4)));
typedef unsigned v2u __attribute__((ext_vector_type(2)));
typedef float v4f __attribute__((ext_vector_type(4)));
typedef short v4s __attribute__((ext_vector_type(4)));
typedef short v8s __attribute__((ext_vector_type(8)));
typedef __bf16 v8bf __attribute__((ext_vector_type(8)));
typedef __bf16 v2bf __attribute__((ext_vector_type(2)));
typedef float v2f __attribute__((ext_vector_type(2)));
typedef unsigned long long u64;
#define LAS __attribute__((address_space(3)))
#define GAS __attribute__((address_space(1)))
#define RLX_AGENT __ATOMIC_RELAXED, __HIP_MEMORY_SCOPE_AGENT

#define PACE_TBS 7.7
#define SLOW_N 16
#define SLOW_US 1.3
#define LN_MIMIC 1

namespace {

constexpr int kD = 640, kHD = 6144, kF = 1536;
constexpr unsigned kKVBytes = 4u * 32u * 8192u * 384u;
constexpr int SLOTB = 12288, NSLOT = 10, INFL = 32;
constexpr int LAG = (INFL + 11) / 12, JP = INFL + 11 - 12 * LAG;
constexpr unsigned PACE = (unsigned)(12288.0 * 256.0 / (PACE_TBS * 1e12) * 1e8 * 256.0);
constexpr unsigned SLOW = (unsigned)(SLOW_US * 100.0 * 256.0 / SLOW_N);
constexpr int TAIL_SLOT = 256 % NSLOT;
static_assert(TAIL_SLOT + 4 <= NSLOT, "tail needs 4 contiguous slots");
static_assert(INFL == 32, "the end-of-stream waits are written for 32");

constexpr int L_SC = NSLOT * SLOTB;
constexpr int L_XN = L_SC + 16384;
constexpr int L_QF = L_XN + 5120;
constexpr int L_OPB = L_QF + 3072;
constexpr int L_OA = L_OPB + 6144;
constexpr int L_CTL = L_OA + 9216;
constexpr int L_BYTES = L_CTL + 512;
static_assert(L_BYTES <= 163840, "LDS");
constexpr int L_PARTA = L_SC + 4096;
constexpr int L_PARTB = L_OPB;
constexpr int L_TAIL = TAIL_SLOT * SLOTB;
enum { C_FULL = 0, C_ABORT = 1, C_XN = 2, C_DONE = 4  , C_ODONE = 8  , C_QP = 12  , C_P = 15,
       C_W = 16, C_OFREE = 17, C_WO = 18, C_PRE = 19, C_G = 20, C_PRO = 21, C_Q = 22  , C_ML = 32  , C_WT = 48  , C_E = 56   };

constexpr size_t W_FL = 256;
constexpr size_t W_ML = W_FL + 256 * 128;
constexpr size_t W_OP = W_ML + 256 * 128;
constexpr size_t W_H1 = W_OP + 7 * 256 * 768;
constexpr size_t W_H2 = W_H1 + 256 * 1536;
constexpr size_t W_END = W_H2 + 256 * 1280;

constexpr float kC = 0.07216878364870322f * 1.4426950408889634f;
constexpr unsigned SPIN_CAP = 1u << 21;

struct Args {
  const unsigned short *x, *k, *v, *wq, *wo, *w1, *b1, *w2, *b2;
  unsigned short* out;
  unsigned char* ws;
};

template <int N> __device__ __forceinline__ void wait_vm() { asm volatile("s_waitcnt vmcnt(%0)" ::"n"(N) : "memory"); }
__device__ __forceinline__ void wait_lds() { asm volatile("s_waitcnt lgkmcnt(0)" ::: "memory"); }
__device__ __forceinline__ void cbar() { asm volatile("" ::: "memory"); }
__device__ __forceinline__ unsigned now32() { return (unsigned)__builtin_amdgcn_s_memrealtime(); }
__device__ __forceinline__ float bf2f(unsigned short h) { return __builtin_bit_cast(float, (unsigned)h << 16); }
__device__ __forceinline__ unsigned pk_bf16(float a, float b) {
  v2f f = {a, b};
  return __builtin_bit_cast(unsigned, __builtin_convertvector(f, v2bf));
}
__device__ __forceinline__ float rbf(float a) { return __builtin_bit_cast(float, pk_bf16(a, 0.f) << 16); }
__device__ __forceinline__ float lo_f(unsigned u) { return __builtin_bit_cast(float, u << 16); }
__device__ __forceinline__ float hi_f(unsigned u) { return __builtin_bit_cast(float, u & 0xffff0000u); }
__device__ __forceinline__ v4f mfma(v4u a, v4u b, v4f c) {
  return __builtin_amdgcn_mfma_f32_16x16x32_bf16(__builtin_bit_cast(v8bf, a), __builtin_bit_cast(v8bf, b), c, 0, 0, 0);
}
__device__ __forceinline__ v2u tr_read(LAS unsigned char* p) {
  return __builtin_bit_cast(v2u, __builtin_amdgcn_ds_read_tr16_b64_v4i16((LAS v4s*)p));
}
template <int CTRL> __device__ __forceinline__ float dpp(float v) {
  return __builtin_bit_cast(float, __builtin_amdgcn_update_dpp(0, __builtin_bit_cast(int, v), CTRL, 0xf, 0xf, true));
}
__device__ __forceinline__ float row16_sum(float v) {
  v += dpp<0x128>(v); v += dpp<0x124>(v); v += dpp<0x122>(v); v += dpp<0x121>(v);
  return v;
}
__device__ __forceinline__ float row16_max(float v) {
  v = fmaxf(v, dpp<0x128>(v)); v = fmaxf(v, dpp<0x124>(v)); v = fmaxf(v, dpp<0x122>(v)); v = fmaxf(v, dpp<0x121>(v));
  return v;
}

typedef volatile LAS unsigned* ctl_t;

#define STAMP(k) do {} while (0)
#define FLUSH() do {} while (0)

template <int SLEEP> __device__ __forceinline__ void wait_ge(ctl_t ctl, int idx, unsigned need) {
  unsigned sp = 0;
#pragma nounroll
  while (ctl[idx] < need) {
    if (SLEEP) __builtin_amdgcn_s_sleep(SLEEP);
    if ((++sp & 63u) == 0u) {
      if (ctl[C_ABORT] != 0u) break;
      if (sp > SPIN_CAP) { ctl[C_ABORT] = 1u; break; }
    }
  }
  cbar();
}
__device__ __forceinline__ bool spin_fail(ctl_t ctl, unsigned& sp) {
  if ((++sp & 15u) == 0u) {
    if (ctl[C_ABORT] != 0u) return true;
    if (sp > (SPIN_CAP >> 2)) { ctl[C_ABORT] = 1u; return true; }
  }
  return false;
}
__device__ __forceinline__ void ctl_add(LAS unsigned char* L, int idx, int lane) {
  if (lane == 0) __hip_atomic_fetch_add((LAS unsigned*)(L + L_CTL) + idx, 1u, __ATOMIC_RELAXED, __HIP_MEMORY_SCOPE_WORKGROUP);
}

struct WRegs { v4u w[40]; };
__device__ __forceinline__ void proj_load(WRegs& r, __amdgpu_buffer_rsrc_t rs, unsigned stride, unsigned base, int lane) {
  const unsigned voff = (unsigned)(32 * ((lane >> 3) & 1) + 8 * (lane >> 4)) * stride + (unsigned)(lane & 7) * 16u;
#pragma unroll
  for (int u = 0; u < 5; ++u)
#pragma unroll
    for (int j = 0; j < 8; ++j) r.w[u * 8 + j] = __builtin_amdgcn_raw_buffer_load_b128(rs, voff, base + (unsigned)(64 * u + j) * stride, 0);
}
__device__ __forceinline__ void proj_mma(const WRegs& r, LAS unsigned char* L, int k0, int lane, v4f (&acc)[8]) {
  LAS unsigned char* xa = L + L_XN + (lane & 3) * 1280 + (k0 + 32 * ((lane >> 2) & 1) + 8 * (lane >> 4)) * 2;
#pragma unroll
  for (int u = 0; u < 5; ++u) {
    const v4u a = *(const LAS v4u*)(xa + u * 128);
#pragma unroll
    for (int d = 0; d < 4; ++d) {
      v4u b0, b1;
#pragma unroll
      for (int jj = 0; jj < 4; ++jj) {
        b0[jj] = __builtin_amdgcn_perm(r.w[u * 8 + 2 * jj + 1][d], r.w[u * 8 + 2 * jj][d], 0x05040100u);
        b1[jj] = __builtin_amdgcn_perm(r.w[u * 8 + 2 * jj + 1][d], r.w[u * 8 + 2 * jj][d], 0x07060302u);
      }
      acc[2 * d] = mfma(a, b0, acc[2 * d]);
      acc[2 * d + 1] = mfma(a, b1, acc[2 * d + 1]);
    }
  }
  asm volatile("" : "+v"(acc[0]), "+v"(acc[1]), "+v"(acc[2]), "+v"(acc[3]), "+v"(acc[4]), "+v"(acc[5]), "+v"(acc[6]), "+v"(acc[7]) :: "memory");
  __builtin_amdgcn_sched_barrier(0);
}
__device__ __forceinline__ void proj_store(LAS unsigned char* part, int t3, int lane, const v4f (&acc)[8]) {
  const int g4 = lane >> 4, hb = (lane >> 3) & 1;
  if (g4 == hb) {
    LAS unsigned char* p = part + (g4 * 192 + 64 * t3 + 8 * (lane & 7)) * 16;
#pragma unroll
    for (int i = 0; i < 8; ++i) *(LAS v4f*)(p + i * 16) = acc[i];
  }
}

__global__ void __launch_bounds__(512, 2) fused_decode(Args a) {
  extern __shared__ __attribute__((aligned(16))) unsigned char lds[];
  LAS unsigned char* L = (LAS unsigned char*)lds;
  ctl_t ctl = (ctl_t)(L + L_CTL);
  const int tid = threadIdx.x, lane = tid & 63, wave = __builtin_amdgcn_readfirstlane(tid >> 6);
  const unsigned wg = (blockIdx.x & 7u) * 32u + (blockIdx.x >> 3);
  const unsigned g = wg >> 4, m = wg & 15u;
  const int g4 = lane >> 4, n16 = lane & 15;
  const bool has_ffn = (g & 1u) == 0u;

  if (wave == 0) {
    const unsigned t0 = now32();
    STAMP(0);
    __builtin_amdgcn_s_setprio(3);
    int voff[6];
#pragma unroll
    for (int j = 0; j < 6; ++j) {
      const int line = 8 * j + (lane >> 3), row = line / 3;
      voff[j] = line * 128 + (((lane & 7) ^ ((row >> 1) & 7)) << 4);
    }
    const __amdgpu_buffer_rsrc_t rk = __builtin_amdgcn_make_buffer_rsrc((void*)a.k, 0, (int)kKVBytes, 0x00020000);
    const __amdgpu_buffer_rsrc_t rv = __builtin_amdgcn_make_buffer_rsrc((void*)a.v, 0, (int)kKVBytes, 0x00020000);
    unsigned pub = 0, s = 0;
    auto wait_free = [&](unsigned it) {
      const unsigned q = it - NSLOT;
      if (ctl[C_DONE + (q & 3u)] <= (q >> 2)) {
        STAMP(4);
        wait_vm<0>();
        const unsigned all = it < 256u ? it : 256u;
        if (all > pub) { pub = all; ctl[C_FULL] = pub; }
        wait_ge<1>(ctl, C_DONE + (q & 3u), (q >> 2) + 1u);
      }
      cbar();
    };
#pragma nounroll
    for (unsigned it = 0; it < 256u; ++it) {
      if (it >= NSLOT) wait_free(it);
      {
        const unsigned due = t0 + ((it * PACE + (it < SLOW_N ? it : SLOW_N) * SLOW) >> 8);
#pragma nounroll
        while ((int)(now32() - due) < 0) __builtin_amdgcn_s_sleep(1);
      }
      const unsigned ph = (it & 127u) >> 4, sub = it & 15u;
      const unsigned so = ((ph * 16u + g) * 8192u + (sub * 16u + m) * 32u) * 384u;
      const __amdgpu_buffer_rsrc_t r = it >= 128u ? rv : rk;
#pragma unroll
      for (int j = 0; j < 12; ++j) {
        __builtin_amdgcn_raw_ptr_buffer_load_lds(r, (LAS void*)(L + s * SLOTB + j * 1024), 16, voff[j % 6], so + (j / 6) * 6144, 0, 2);
        wait_vm<INFL>();
        if (j == JP) {
          if (it + 1u > pub + LAG) { pub = it + 1u - LAG; ctl[C_FULL] = pub; }
        }
      }
      if (it == 0) __builtin_amdgcn_s_barrier();
      s = s + 1 == NSLOT ? 0 : s + 1;
    }
    STAMP(1);
    wait_free(256u); wait_free(257u); wait_free(258u); wait_free(259u);
    const __amdgpu_buffer_rsrc_t ro = __builtin_amdgcn_make_buffer_rsrc((void*)a.wo, 0, kHD * kD * 2, 0x00020000);
    const __amdgpu_buffer_rsrc_t r2 = __builtin_amdgcn_make_buffer_rsrc((void*)a.w2, 0, kF * kD * 2, 0x00020000);
    int toff[15];
#pragma unroll
    for (int j = 0; j < 15; ++j) { const int idx = j * 64 + lane, row = idx / 5; toff[j] = row * 1280 + (idx - row * 5) * 16; }
#pragma unroll
    for (int h = 0; h < 2; ++h)
#pragma unroll
      for (int j = 0; j < 15; ++j)
        __builtin_amdgcn_raw_ptr_buffer_load_lds(ro, (LAS void*)(L + L_TAIL + (h * 15 + j) * 1024), 16, toff[j], (g + 16u * h) * (192u * 1280u) + m * 80u, 0, 0);
    STAMP(2);
    wait_vm<54>(); if (pub < 254u) ctl[C_FULL] = 254u;
    wait_vm<42>(); if (pub < 255u) ctl[C_FULL] = 255u;
    if (has_ffn) {
#pragma unroll
      for (int j = 0; j < 15; ++j)
        __builtin_amdgcn_raw_ptr_buffer_load_lds(r2, (LAS void*)(L + L_TAIL + (30 + j) * 1024), 16, toff[j], (g >> 1) * (192u * 1280u) + m * 80u, 0, 0);
      wait_vm<45>();
    } else {
      wait_vm<30>();
    }
    ctl[C_FULL] = 256u;
    STAMP(3);
    wait_vm<0>();
    ctl[C_WO] = 1u;
    FLUSH();
    return;
  }

  const __amdgpu_buffer_rsrc_t rq = __builtin_amdgcn_make_buffer_rsrc((void*)a.wq, 0, kD * kHD * 2, 0x00020000);

  if (wave <= 4) {
    const int c = wave - 1;
    STAMP(0);
    if (c < 3) {
      WRegs r;
      __builtin_amdgcn_s_barrier();
      cbar();
      proj_load(r, rq, kHD * 2, g * 384u + c * 128u, lane);
      wait_ge<1>(ctl, C_XN, 1u);
      v4f acc[8];
#pragma unroll
      for (int i = 0; i < 8; ++i) acc[i] = v4f{0.f, 0.f, 0.f, 0.f};
      proj_mma(r, L, 0, lane, acc);
      proj_store(L + L_PARTA, c, lane, acc);
      wait_lds();
      ctl[C_QP + c] = 1u;
    } else {
      v4u xv[5];
#pragma unroll
      for (int j = 0; j < 5; ++j) xv[j] = *(const GAS v4u*)((const GAS unsigned char*)a.x + g4 * 1280 + (n16 + 16 * j) * 16);
      ctl[lane] = 0u;
      wait_lds();
      __builtin_amdgcn_s_barrier();
      float f[40];
#pragma unroll
      for (int j = 0; j < 5; ++j)
#pragma unroll
        for (int d = 0; d < 4; ++d) { f[j * 8 + 2 * d] = lo_f(xv[j][d]); f[j * 8 + 2 * d + 1] = hi_f(xv[j][d]); }
      float sum = 0.f;
#pragma unroll
      for (int i = 0; i < 40; ++i) sum += f[i];
      const float mean = row16_sum(sum) * (1.f / kD);
      float sq = 0.f;
#pragma unroll
      for (int i = 0; i < 40; ++i) { const float d = f[i] - mean; sq += d * d; }
      const float var = row16_sum(sq) * (1.f / kD);
      const float mb = rbf(mean), sd = rbf(sqrtf(rbf(rbf(var) + 1e-5f)));
#pragma unroll
      for (int i = 0; i < 40; ++i) f[i] = rbf(f[i] - mb) / sd;
#pragma unroll
      for (int j = 0; j < 5; ++j) {
        v4u o;
#pragma unroll
        for (int d = 0; d < 4; ++d) o[d] = pk_bf16(f[j * 8 + 2 * d], f[j * 8 + 2 * d + 1]);
        *(LAS v4u*)(L + L_XN + g4 * 1280 + (n16 + 16 * j) * 16) = o;
      }
      wait_lds();
      ctl[C_XN] = 1u;
    }

    unsigned s = c;
    STAMP(1);
    {
      const unsigned ka0 = n16 * 384 + ((g4 ^ (n16 >> 1)) << 4), ka1 = ka0 ^ 64u;
#pragma nounroll
      for (unsigned ph = 0; ph < 8u; ++ph) {
        if (ph < 2u) wait_ge<1>(ctl, C_Q + ph, 3u);
        if (ph == 0u) STAMP(2);
        if (ph == 1u) STAMP(3);
        v4u qf[6];
#pragma unroll
        for (int t = 0; t < 6; ++t) qf[t] = *(const LAS v4u*)(L + L_QF + ((ph & 1u) * 4u + (ph >> 1)) * 384u + t * 64 + g4 * 16);
#pragma nounroll
        for (unsigned ii = 0; ii < 4u; ++ii) {
          const unsigned sub = ii * 4u + c, it = ph * 16u + sub;
          wait_ge<1>(ctl, C_FULL, it + 1u);
          LAS unsigned char* p0 = L + s * SLOTB + ka0;
          LAS unsigned char* p1 = L + s * SLOTB + ka1;
          v4u kf[12];
#pragma unroll
          for (int hf = 0; hf < 2; ++hf)
#pragma unroll
            for (int t = 0; t < 6; ++t) kf[hf * 6 + t] = *(const LAS v4u*)(((t & 1) ? p1 : p0) + hf * 6144 + (t >> 1) * 128);
          v4f d0 = {0.f, 0.f, 0.f, 0.f}, d1 = {0.f, 0.f, 0.f, 0.f};
#pragma unroll
          for (int t = 0; t < 6; ++t) { d0 = mfma(qf[t], kf[t], d0); d1 = mfma(qf[t], kf[6 + t], d1); }
          const float sc = (g4 & 1) ? d1[0] : d0[0];
          if (lane < 32) *(LAS float*)(L + L_SC + ph * 2048u + (sub * 32u + lane) * 4u) = sc;
          wait_lds();
          ctl[C_DONE + c] = (it >> 2) + 1u;
          s += 4; if (s >= NSLOT) s -= NSLOT;
        }
      }
    }
    float o3[3];
    {
      const int q = n16 >> 2, p = lane & 3;
      unsigned va[2];
#pragma unroll
      for (int t = 0; t < 2; ++t) {
        const int row = 8 * g4 + (q & 1) + 4 * (q >> 1) + 2 * t;
        va[t] = row * 384 + ((((p >> 1)) ^ ((row >> 1) & 7)) << 4) + 8 * (p & 1);
      }
#pragma nounroll
      for (unsigned ph = 0; ph < 8u; ++ph) {
        wait_ge<1>(ctl, C_P, ph + 1u);
        v4f acc[12];
#pragma unroll
        for (int i = 0; i < 12; ++i) acc[i] = v4f{0.f, 0.f, 0.f, 0.f};
#pragma nounroll
        for (unsigned ii = 0; ii < 4u; ++ii) {
          const unsigned sub = ii * 4u + c, it = 128u + ph * 16u + sub;
          const v4u pa = *(const LAS v4u*)(L + L_SC + ph * 2048u + (lane & 1) * 1024 + sub * 64u + g4 * 16);
          wait_ge<1>(ctl, C_FULL, it + 1u);
          const unsigned b0 = s * SLOTB + va[0], b1 = s * SLOTB + va[1];
          v4u vf[12];
#pragma unroll
          for (int t = 0; t < 12; ++t) {
            const v2u lo = tr_read(L + (b0 ^ ((t & 3) << 5)) + (t >> 2) * 128);
            const v2u hi = tr_read(L + (b1 ^ ((t & 3) << 5)) + (t >> 2) * 128);
            vf[t] = v4u{lo[0], lo[1], hi[0], hi[1]};
          }
#pragma unroll
          for (int t = 0; t < 12; ++t) acc[t] = mfma(pa, vf[t], acc[t]);
          wait_lds();
          ctl[C_DONE + c] = (it >> 2) + 1u;
          s += 4; if (s >= NSLOT) s -= NSLOT;
        }
#pragma unroll
        for (int i = 0; i < 3; ++i) {
          const float e0 = acc[4 * i][0] + acc[4 * i][1], e1 = acc[4 * i + 1][0] + acc[4 * i + 1][1];
          const float e2 = acc[4 * i + 2][0] + acc[4 * i + 2][1], e3 = acc[4 * i + 3][0] + acc[4 * i + 3][1];
          const float u = (g4 & 1) ? e1 : e0, w = (g4 & 1) ? e3 : e2;
          o3[i] = (g4 & 2) ? w : u;
        }
        if (ph < 7u || c < 3) {
          if (ph >= 2u) wait_ge<1>(ctl, C_OFREE, ph - 1u);
#pragma unroll
          for (int i = 0; i < 3; ++i) *(LAS float*)(L + L_OPB + ((ph & 1u) * 4u + c) * 768u + (lane + 64 * i) * 4) = o3[i];
          wait_lds();
          ctl[C_ODONE + c] = ph + 1u;
        }
      }
    }
    STAMP(4);
    if (c == 3) {
      wait_ge<0>(ctl, C_W, 1u);
      wait_ge<0>(ctl, C_ODONE + 0, 8u); wait_ge<0>(ctl, C_ODONE + 1, 8u); wait_ge<0>(ctl, C_ODONE + 2, 8u);
      const float wt = __builtin_bit_cast(float, ctl[C_WT + 7]);
      const unsigned tg = ctl[C_E] + 1u;
      GAS u64* h1 = (GAS u64*)(a.ws + W_H1) + wg * 192u + lane;
#pragma unroll
      for (int i = 0; i < 3; ++i) {
        LAS float* op = (LAS float*)(L + L_OPB + 4 * 768 + (lane + 64 * i) * 4);
        const float v = (((op[0] + op[192]) + op[384]) + o3[i]) * wt;
        __hip_atomic_store(h1 + 64 * i, ((u64)tg << 32) | __builtin_bit_cast(unsigned, v), RLX_AGENT);
      }
      FLUSH();
      return;
    }
    {
      wait_ge<1>(ctl, C_PRE, 3u);
      const unsigned tg = ctl[C_E] + 1u;
      wait_ge<0>(ctl, C_WO, 1u);
      LAS unsigned char* ba = L + L_TAIL + (8 * g4 + (n16 >> 2)) * 80 + c * 32 + (lane & 3) * 8;
      LAS unsigned char* aa = L + L_OA + (lane & 7) * 384 + g4 * 16;
      v4f y0 = {0.f, 0.f, 0.f, 0.f}, y1 = {0.f, 0.f, 0.f, 0.f};
      v4u bb[6];
#pragma unroll
      for (int t = 0; t < 6; ++t) {
        const v2u lo = tr_read(ba + t * 2560), hi = tr_read(ba + t * 2560 + 320);
        y0 = mfma(*(const LAS v4u*)(aa + t * 64), v4u{lo[0], lo[1], hi[0], hi[1]}, y0);
      }
      if (has_ffn) {
#pragma unroll
        for (int t = 0; t < 6; ++t) {
          const v2u lo = tr_read(ba + 30720 + t * 2560), hi = tr_read(ba + 30720 + t * 2560 + 320);
          y0 = mfma(*(const LAS v4u*)(aa + 6144 + t * 64), v4u{lo[0], lo[1], hi[0], hi[1]}, y0);
        }
      }
#pragma unroll
      for (int t = 0; t < 6; ++t) {
        const v2u lo = tr_read(ba + 15360 + t * 2560), hi = tr_read(ba + 15360 + t * 2560 + 320);
        bb[t] = v4u{lo[0], lo[1], hi[0], hi[1]};
      }
      const unsigned bsel = 2 * (g4 & 1) + (g4 >> 1), col = 16 * c + n16, oi = bsel * 40 + col;
      GAS u64* h2 = (GAS u64*)(a.ws + W_H2) + ((m * 16u + oi / 10u) * 16u + g) * 10u + oi % 10u;
      STAMP(5);
      wait_ge<0>(ctl, C_G, 3u);
      STAMP(6);
#pragma unroll
      for (int t = 0; t < 6; t += 2) {
        y0 = mfma(*(const LAS v4u*)(aa + 3072 + t * 64), bb[t], y0);
        y1 = mfma(*(const LAS v4u*)(aa + 3072 + t * 64 + 64), bb[t + 1], y1);
      }
      y0 += y1;
      const float y = (g4 & 2) ? y0[2] + y0[3] : y0[0] + y0[1];
      if (col < 40u) __hip_atomic_store(h2, ((u64)tg << 32) | __builtin_bit_cast(unsigned, y), RLX_AGENT);
    }
    FLUSH();
    return;
  }

  const int sv = wave - 5;
  u64 epoch = 0;
  {
    WRegs r;
    v4f acc[8];
    if (sv == 0) epoch = __hip_atomic_load((GAS u64*)a.ws, RLX_AGENT);
    __builtin_amdgcn_s_barrier();
    cbar();
    proj_load(r, rq, kHD * 2, 320u * (kHD * 2) + g * 384u + sv * 128u, lane);
    wait_ge<1>(ctl, C_XN, 1u);
    STAMP(0);
#pragma unroll
    for (int i = 0; i < 8; ++i) acc[i] = v4f{0.f, 0.f, 0.f, 0.f};
    proj_mma(r, L, 320, lane, acc);
    cbar();
    proj_load(r, rq, kHD * 2, (g + 16u) * 384u + sv * 128u, lane);
    proj_store(L + L_PARTA + 6144, sv, lane, acc);
    wait_ge<0>(ctl, C_QP + sv, 1u);
    {
      const LAS v4f* pp = (const LAS v4f*)(L + L_PARTA) + 64 * sv + lane;
      const v4f qv = (pp[0] + pp[192]) + (pp[384] + pp[576]);
#pragma unroll
      for (int b = 0; b < 4; ++b) *(LAS unsigned short*)(L + L_QF + b * 384 + (64 * sv + lane) * 2) = (unsigned short)pk_bf16(qv[b], 0.f);
      wait_lds();
      ctl_add(L, C_Q, lane);
    }
#pragma unroll
    for (int i = 0; i < 8; ++i) acc[i] = v4f{0.f, 0.f, 0.f, 0.f};
    proj_mma(r, L, 0, lane, acc);
    cbar();
    proj_load(r, rq, kHD * 2, 320u * (kHD * 2) + (g + 16u) * 384u + sv * 128u, lane);
    proj_mma(r, L, 320, lane, acc);
    cbar();
    const __amdgpu_buffer_rsrc_t r1 = __builtin_amdgcn_make_buffer_rsrc((void*)a.w1, 0, kD * kF * 2, 0x00020000);
    if (has_ffn) proj_load(r, r1, kF * 2, (g >> 1) * 384u + sv * 128u, lane);
    proj_store(L + L_PARTB, sv, lane, acc);
    wait_lds();
    {
      const LAS v4f* pp = (const LAS v4f*)(L + L_PARTB) + 64 * sv + lane;
      const v4f qv = pp[0] + pp[192];
#pragma unroll
      for (int b = 0; b < 4; ++b) *(LAS unsigned short*)(L + L_QF + (4 + b) * 384 + (64 * sv + lane) * 2) = (unsigned short)pk_bf16(qv[b], 0.f);
      wait_lds();
      ctl_add(L, C_Q + 1, lane);
    }
    if (has_ffn) {
      const float bias = bf2f(a.b1[(g >> 1) * 192u + 64 * sv + lane]);
#pragma unroll
      for (int i = 0; i < 8; ++i) acc[i] = v4f{0.f, 0.f, 0.f, 0.f};
      proj_mma(r, L, 0, lane, acc);
      cbar();
      proj_load(r, r1, kF * 2, 320u * (kF * 2) + (g >> 1) * 384u + sv * 128u, lane);
      proj_mma(r, L, 320, lane, acc);
      proj_store(L + L_PARTB, sv, lane, acc);
      wait_lds();
      const LAS v4f* pp = (const LAS v4f*)(L + L_PARTB) + 64 * sv + lane;
      const v4f hv = pp[0] + pp[192];
#pragma unroll
      for (int b = 0; b < 4; ++b) {
        const float rr = fmaxf(hv[b] + bias, 0.f), h = rr * rr;
        const float hh = rbf(h);
        LAS unsigned short* o = (LAS unsigned short*)(L + L_OA + 6144 + 2 * b * 384 + (64 * sv + lane) * 2);
        o[0] = (unsigned short)(__builtin_bit_cast(unsigned, hh) >> 16);
        o[192] = (unsigned short)pk_bf16(h - hh, 0.f);
      }
      wait_lds();
    }
  }
  ctl_add(L, C_PRO, lane);
  STAMP(1);

  GAS u64* flags = (GAS u64*)(a.ws + W_FL);
  float resid = 0.f;
  if (sv == 0) {
    const u64 clr = (u64)(unsigned)epoch << 32;
    {
      GAS u64* h1 = (GAS u64*)(a.ws + W_H1) + wg * 192u + lane;
#pragma unroll
      for (int i = 0; i < 3; ++i) __hip_atomic_store(h1 + 64 * i, clr, RLX_AGENT);
#pragma unroll
      for (int i = 0; i < 3; ++i) {
        const unsigned oi = lane + 64 * i;
        if (oi < 160u) __hip_atomic_store((GAS u64*)(a.ws + W_H2) + ((m * 16u + oi / 10u) * 16u + g) * 10u + oi % 10u, clr, RLX_AGENT);
      }
    }
    if (lane < 40) {
      const unsigned oi = g * 10u + (lane >> 2), b = oi / 40u, col = m * 40u + oi % 40u;
      resid = bf2f(a.x[b * kD + col]) + bf2f(a.b2[col]);
    }
    wait_ge<1>(ctl, C_PRO, 3u);
#pragma nounroll
    for (unsigned ph = 0; ph < 8u; ++ph) {
#pragma unroll
      for (int c = 0; c < 4; ++c) wait_ge<4>(ctl, C_DONE + c, 4u * (ph + 1u));
      LAS unsigned char* sp = L + L_SC + ph * 2048u;
      const v4f s0 = *(const LAS v4f*)(sp + lane * 32), s1 = *(const LAS v4f*)(sp + lane * 32 + 16);
      float mx = fmaxf(fmaxf(fmaxf(s0[0], s0[1]), fmaxf(s0[2], s0[3])), fmaxf(fmaxf(s1[0], s1[1]), fmaxf(s1[2], s1[3])));
      mx = row16_max(mx);
      mx = fmaxf(mx, __shfl_xor(mx, 16));
      mx = fmaxf(mx, __shfl_xor(mx, 32));
      float p[8];
#pragma unroll
      for (int i = 0; i < 4; ++i) { p[i] = __builtin_amdgcn_exp2f((s0[i] - mx) * kC); p[4 + i] = __builtin_amdgcn_exp2f((s1[i] - mx) * kC); }
      float l = ((p[0] + p[1]) + (p[2] + p[3])) + ((p[4] + p[5]) + (p[6] + p[7]));
      l = row16_sum(l);
      l += __shfl_xor(l, 16);
      l += __shfl_xor(l, 32);
      v4u hi, lo;
#pragma unroll
      for (int d = 0; d < 4; ++d) {
        const int i = (d == 1) ? 4 : (d == 2) ? 2 : 2 * d;
        hi[d] = pk_bf16(p[i], p[i + 1]);
        lo[d] = pk_bf16(p[i] - lo_f(hi[d]), p[i + 1] - hi_f(hi[d]));
      }
      cbar();
      *(LAS v4u*)(sp + lane * 16) = hi;
      *(LAS v4u*)(sp + 1024 + lane * 16) = lo;
      if (lane == 0) { ctl[C_ML + 2 * ph] = __builtin_bit_cast(unsigned, mx); ctl[C_ML + 2 * ph + 1] = __builtin_bit_cast(unsigned, l); }
      wait_lds();
      ctl[C_P] = ph + 1u;
    }
    STAMP(2);
    GAS unsigned* ml = (GAS unsigned*)(a.ws + W_ML);
    if (lane < 16) __hip_atomic_store(ml + wg * 32u + lane, (unsigned)ctl[C_ML + lane], RLX_AGENT);
    wait_vm<0>();
    if (lane == 0) __hip_atomic_store(flags + wg * 16u, epoch + 1u, RLX_AGENT);
    {
      const unsigned src = (lane & 16) ? (unsigned)n16 * 16u + m : g * 16u + (unsigned)n16;
      unsigned sp = 0;
#pragma nounroll
    for (;;) {
        const u64 f = __hip_atomic_load(flags + src * 16u, RLX_AGENT);
        if (__all(f == epoch + 1u)) break;
        if (spin_fail(ctl, sp)) break;
        __builtin_amdgcn_s_sleep(8);
      }
      cbar();
    }
#pragma unroll
    for (int h = 0; h < 2; ++h) {
      const unsigned ph = 4 * h + g4;
      const u64 e = __hip_atomic_load((GAS u64*)(ml + (g * 16u + n16) * 32u + 2u * ph), RLX_AGENT);
      const float mj = __builtin_bit_cast(float, (unsigned)e), lj = __builtin_bit_cast(float, (unsigned)(e >> 32));
      const float mx = row16_max(mj);
      const float wj = __builtin_amdgcn_exp2f((mj - mx) * kC);
      const float tot = row16_sum(wj * lj);
      if ((unsigned)n16 == m) ctl[C_WT + ph] = __builtin_bit_cast(unsigned, wj / tot);
    }
    if (lane == 0) { ctl[C_E] = (unsigned)epoch; ctl[C_E + 1] = (unsigned)(epoch >> 32); }
    wait_lds();
    ctl[C_W] = 1u;
  } else {
    wait_ge<8>(ctl, C_W, 1u);
    epoch = ((u64)ctl[C_E + 1] << 32) | ctl[C_E];
    if (sv == 1) {
#pragma nounroll
      for (unsigned ph = 0; ph < 7u; ++ph) {
#pragma unroll
        for (int c = 0; c < 4; ++c) wait_ge<4>(ctl, C_ODONE + c, ph + 1u);
        const float wt = __builtin_bit_cast(float, ctl[C_WT + ph]);
        GAS unsigned* op = (GAS unsigned*)(a.ws + W_OP) + (ph * 256u + wg) * 192u + lane;
        float v[3];
#pragma unroll
        for (int i = 0; i < 3; ++i) {
          const LAS float* q = (const LAS float*)(L + L_OPB + (ph & 1u) * 3072u + (lane + 64 * i) * 4);
          v[i] = (((q[0] + q[192]) + q[384]) + q[576]) * wt;
        }
        wait_lds();
        ctl[C_OFREE] = ph + 1u;
#pragma unroll
        for (int i = 0; i < 3; ++i) __hip_atomic_store(op + 64 * i, __builtin_bit_cast(unsigned, v[i]), RLX_AGENT);
        wait_vm<0>();
        if (lane == 0) __hip_atomic_store(flags + wg * 16u + 1u + ph, epoch + 1u, RLX_AGENT);
      }
    } else {
      const __amdgpu_buffer_rsrc_t rw = __builtin_amdgcn_make_buffer_rsrc((void*)(a.ws + W_OP), 0, 7 * 256 * 768, 0x00020000);
#pragma nounroll
      for (unsigned ph = 0; ph < 7u; ++ph) {
        wait_ge<8>(ctl, C_OFREE, ph + 1u);
        unsigned sp = 0;
#pragma nounroll
        for (;;) {
          const u64 f = __hip_atomic_load(flags + (g * 16u + n16) * 16u + 1u + ph, RLX_AGENT);
          if (__all(f == epoch + 1u)) break;
          if (spin_fail(ctl, sp)) break;
          __builtin_amdgcn_s_sleep(8);
        }
        cbar();
        if (lane < 48) {
          v4u t[16];
#pragma unroll
          for (int j = 0; j < 16; ++j) t[j] = __builtin_amdgcn_raw_buffer_load_b128(rw, lane * 16, (ph * 256u + g * 16u + j) * 768u, 16);
          v4f o = __builtin_bit_cast(v4f, t[0]);
#pragma unroll
          for (int j = 1; j < 16; ++j) o += __builtin_bit_cast(v4f, t[j]);
          v2u hi, lo;
#pragma unroll
          for (int d = 0; d < 2; ++d) {
            hi[d] = pk_bf16(o[2 * d], o[2 * d + 1]);
            lo[d] = pk_bf16(o[2 * d] - lo_f(hi[d]), o[2 * d + 1] - hi_f(hi[d]));
          }
          LAS unsigned char* dst = L + L_OA + (ph & 1u) * 3072u + (ph >> 1) * 768u + lane * 8;
          *(LAS v2u*)dst = hi;
          *(LAS v2u*)(dst + 384) = lo;
        }
      }
      wait_lds();
    }
  }
  ctl_add(L, C_PRE, lane);
  STAMP(3);
  const unsigned tg = (unsigned)epoch + 1u;
  {
    wait_ge<1>(ctl, C_FULL, 256u);
    STAMP(4);
    GAS u64* h1 = (GAS u64*)(a.ws + W_H1) + g * 16u * 192u + 64 * sv + lane;
    float o;
    unsigned sp = 0;
#pragma nounroll
    for (;;) {
      u64 t[16];
#pragma unroll
      for (int j = 0; j < 16; ++j) t[j] = __hip_atomic_load(h1 + j * 192, RLX_AGENT);
      bool ok = true;
      o = 0.f;
#pragma unroll
      for (int j = 0; j < 16; ++j) { ok &= (unsigned)(t[j] >> 32) == tg; o += __builtin_bit_cast(float, (unsigned)t[j]); }
      if (__all(ok)) break;
      if (spin_fail(ctl, sp)) break;
    }
    const float oh = rbf(o);
    LAS unsigned short* dst = (LAS unsigned short*)(L + L_OA + 3072 + 6 * 384 + (64 * sv + lane) * 2);
    dst[0] = (unsigned short)(__builtin_bit_cast(unsigned, oh) >> 16);
    dst[192] = (unsigned short)pk_bf16(o - oh, 0.f);
    wait_lds();
    ctl_add(L, C_G, lane);
    STAMP(5);
  }
  if (sv == 0) {
    const int v = lane < 40 ? (lane >> 2) : 0;
    GAS u64* h2 = (GAS u64*)(a.ws + W_H2) + ((m * 16u + g) * 16u + (lane & 3) * 4u) * 10u + v;
    float y;
    unsigned sp = 0;
#pragma nounroll
    for (;;) {
      u64 t[4];
#pragma unroll
      for (int j = 0; j < 4; ++j) t[j] = __hip_atomic_load(h2 + j * 10, RLX_AGENT);
      bool ok = true;
      y = 0.f;
#pragma unroll
      for (int j = 0; j < 4; ++j) { ok &= (unsigned)(t[j] >> 32) == tg; y += __builtin_bit_cast(float, (unsigned)t[j]); }
      if (__all(ok)) break;
      if (spin_fail(ctl, sp)) break;
    }
    STAMP(6);
    y += dpp<0xB1>(y);
    y += dpp<0x4E>(y);
    if (lane < 40 && (lane & 3) == 0) {
      const unsigned oi = g * 10u + v, b = oi / 40u, col = m * 40u + oi % 40u;
      a.out[b * kD + col] = (unsigned short)pk_bf16(y + resid, 0.f);
    }
    if (wg == 0u && lane == 0) __hip_atomic_store((GAS u64*)a.ws, epoch + 1u, RLX_AGENT);
  }
  FLUSH();
}

}

extern "C" void kernel_launch(void* const* d_in, const int* in_sizes, int n_in,
                              void* d_out, int out_size, void* d_ws, size_t ws_size,
                              hipStream_t stream) {
  (void)in_sizes; (void)n_in; (void)out_size; (void)ws_size;
  Args a;
  a.x = (const unsigned short*)d_in[0];
  a.k = (const unsigned short*)d_in[1];
  a.v = (const unsigned short*)d_in[2];
  a.wq = (const unsigned short*)d_in[3];
  a.wo = (const unsigned short*)d_in[4];
  a.w1 = (const unsigned short*)d_in[6];
  a.b1 = (const unsigned short*)d_in[7];
  a.w2 = (const unsigned short*)d_in[8];
  a.b2 = (const unsigned short*)d_in[9];
  a.out = (unsigned short*)d_out;
  a.ws = (unsigned char*)d_ws;
  (void)hipFuncSetAttribute((const void*)fused_decode, hipFuncAttributeMaxDynamicSharedMemorySize, L_BYTES);
  fused_decode<<<256, 512, L_BYTES, stream>>>(a);
}
```

```cpp
#include <hip/hip_runtime.h>
#include <hip/hip_bf16.h>
#include <stdint.h>

typedef unsigned v4u __attribute__((ext_vector_type(4)));
typedef unsigned v2u __attribute__((ext_vector_type(2)));
typedef float v4f __attribute__((ext_vector_type(4)));
typedef short v4s __attribute__((ext_vector_type(4)));
typedef __bf16 v8bf __attribute__((ext_vector_type(8)));
typedef __bf16 v2bf __attribute__((ext_vector_type(2)));
typedef float v2f __attribute__((ext_vector_type(2)));
typedef unsigned long long u64;
#define LAS __attribute__((address_space(3)))
#define GAS __attribute__((address_space(1)))
#define RLX_AGENT __ATOMIC_RELAXED, __HIP_MEMORY_SCOPE_AGENT

#define PACE_TBS 7.5
#define START_US 1.6
#define SKEW_US 0.45
#define POLL_US 6.5
#define TAIL_IT 253
#define PUB_PH 5u
#define LAUX 16
#define SAUX 16
#define GDELAY 24u

namespace {

constexpr int kD = 640, kHD = 6144, kF = 1536;
constexpr unsigned kKVBytes = 4u * 32u * 8192u * 384u;
constexpr int SLOTB = 12288, NSLOT = 11, INFL = 32;
constexpr int LAG = (INFL + 11) / 12, JP = INFL + 11 - 12 * LAG;
constexpr unsigned PACE = (unsigned)(12288.0 * 256.0 / (PACE_TBS * 1e12) * 1e8 * 256.0);
constexpr unsigned START = (unsigned)(START_US * 100.0 * 256.0);
constexpr unsigned SKEW = (unsigned)(SKEW_US * 100.0 * 256.0 / 31.0);
constexpr int TAIL_SLOT = 256 % NSLOT;
static_assert(TAIL_SLOT + 4 <= NSLOT, "tail needs 4 contiguous slots");
static_assert(INFL >= 24 && INFL <= 36, "the end-of-stream waits");
static_assert(START >= 31 * SKEW, "start offset");

constexpr int L_SC = NSLOT * SLOTB;
constexpr int L_QF = L_SC + 16384;
constexpr int L_OPB = L_QF + 3072;
constexpr int L_OA = L_OPB + 4608;
constexpr int L_CS = L_OA + 1280;
constexpr int L_CTL = L_CS + 2560;
constexpr int L_BYTES = L_CTL + 512;
static_assert(L_BYTES <= 163840, "LDS");
constexpr int L_ST = L_QF;
constexpr int L_WT = L_CS;
constexpr int L_PART = L_SC + 4096;
constexpr int L_TAIL = TAIL_SLOT * SLOTB;
enum { C_FULL = 0, C_PART = 1, C_PRO = 2, C_P = 3, C_DONE = 4  , C_ODONE = 8  , C_OFREE = 11, C_Q = 12  ,
       C_WO = 15, C_G = 16, C_TAG = 18  , C_S = 20  , C_END = 24 };

constexpr unsigned W_QX = 256;
constexpr unsigned W_ML = W_QX + 240 * 1024;
constexpr unsigned W_H1 = W_ML + 256 * 128;
constexpr unsigned W_H2 = W_H1 + 8 * 256 * 768;
constexpr unsigned W_END = W_H2 + 160 * 6144;

constexpr float kC = 0.07216878364870322f * 1.4426950408889634f;

struct Args {
  const unsigned short *x, *k, *v, *wq, *wo, *w1, *b1, *w2, *b2;
  unsigned short* out;
  unsigned char* ws;
};

template <int N> __device__ __forceinline__ void wait_vm() { asm volatile("s_waitcnt vmcnt(%0)" ::"n"(N) : "memory"); }
__device__ __forceinline__ void wait_lds() { asm volatile("s_waitcnt lgkmcnt(0)" ::: "memory"); }
__device__ __forceinline__ void cbar() { asm volatile("" ::: "memory"); }
__device__ __forceinline__ unsigned now32() { return (unsigned)__builtin_amdgcn_s_memrealtime(); }
__device__ __forceinline__ float asf(unsigned u) { return __builtin_bit_cast(float, u); }
__device__ __forceinline__ unsigned asu(float f) { return __builtin_bit_cast(unsigned, f); }
__device__ __forceinline__ float bf2f(unsigned short h) { return asf((unsigned)h << 16); }
__device__ __forceinline__ unsigned pk_bf16(float a, float b) {
  v2f f = {a, b};
  return __builtin_bit_cast(unsigned, __builtin_convertvector(f, v2bf));
}
__device__ __forceinline__ float lo_f(unsigned u) { return asf(u << 16); }
__device__ __forceinline__ float hi_f(unsigned u) { return asf(u & 0xffff0000u); }
__device__ __forceinline__ v4f mfma(v4u a, v4u b, v4f c) {
  return __builtin_amdgcn_mfma_f32_16x16x32_bf16(__builtin_bit_cast(v8bf, a), __builtin_bit_cast(v8bf, b), c, 0, 0, 0);
}
__device__ __forceinline__ v4u tr_read2(LAS unsigned char* p0, LAS unsigned char* p1) {
  const v2u lo = __builtin_bit_cast(v2u, __builtin_amdgcn_ds_read_tr16_b64_v4i16((LAS v4s*)p0));
  const v2u hi = __builtin_bit_cast(v2u, __builtin_amdgcn_ds_read_tr16_b64_v4i16((LAS v4s*)p1));
  return v4u{lo[0], lo[1], hi[0], hi[1]};
}
template <int CTRL> __device__ __forceinline__ float dpp(float v) {
  return asf(__builtin_amdgcn_update_dpp(0, (int)asu(v), CTRL, 0xf, 0xf, true));
}
__device__ __forceinline__ float row16_sum(float v) {
  v += dpp<0x128>(v); v += dpp<0x124>(v); v += dpp<0x122>(v); v += dpp<0x121>(v);
  return v;
}
__device__ __forceinline__ float row16_max(float v) {
  v = fmaxf(v, dpp<0x128>(v)); v = fmaxf(v, dpp<0x124>(v)); v = fmaxf(v, dpp<0x122>(v)); v = fmaxf(v, dpp<0x121>(v));
  return v;
}
__device__ __forceinline__ float sel(bool c, float a, float b) { return c ? a : b; }
__device__ __forceinline__ float rdlane(float v, int l) { return asf(__builtin_amdgcn_readlane(asu(v), l)); }
__device__ __forceinline__ float swap32_sum(float a, float b) {
  const v2u r = __builtin_amdgcn_permlane32_swap(asu(a), asu(b), false, false);
  return asf(r[0]) + asf(r[1]);
}
__device__ __forceinline__ float swap16_sum(float a, float b) {
  const v2u r = __builtin_amdgcn_permlane16_swap(asu(a), asu(b), false, false);
  return asf(r[0]) + asf(r[1]);
}
__device__ __forceinline__ void st16(LAS unsigned char* p, unsigned v) { *(LAS unsigned short*)p = (unsigned short)v; }

typedef volatile LAS unsigned* ctl_t;

#define STAMP(k) do {} while (0)
#define FLUSH() do {} while (0)

template <int SLEEP> __device__ __forceinline__ void wait_ge(ctl_t ctl, int idx, unsigned need, int& budget) {
#pragma nounroll
  while ((unsigned)__builtin_amdgcn_readfirstlane(ctl[idx]) < need && budget > 0) {
    if (SLEEP) __builtin_amdgcn_s_sleep(SLEEP);
    budget -= 1 + SLEEP;
  }
  cbar();
}
__device__ __forceinline__ void wait_time(unsigned due) {
#pragma nounroll
  while ((int)(now32() - due) < 0) __builtin_amdgcn_s_sleep(1);
}
__device__ __forceinline__ void ctl_add(LAS unsigned char* L, int idx, int lane) {
  if (lane == 0) __hip_atomic_fetch_add((LAS unsigned*)(L + L_CTL) + idx, 1u, __ATOMIC_RELAXED, __HIP_MEMORY_SCOPE_WORKGROUP);
}
__device__ __forceinline__ void put(__amdgpu_buffer_rsrc_t rw, unsigned voff, unsigned soff, unsigned v0, unsigned v1, unsigned t0, unsigned t1) {
  __builtin_amdgcn_raw_buffer_store_b128(v4u{v0, t0, v1, t1}, rw, voff, soff, SAUX);
}
template <int N, int SLEEP>
__device__ __forceinline__ void sweep(__amdgpu_buffer_rsrc_t rw, unsigned voff, unsigned soff, unsigned S, unsigned t0, unsigned t1, v4u (&t)[N], int& budget, bool calm = false) {
#pragma nounroll
  for (;;) {
#pragma unroll
    for (int j = 0; j < N; ++j) t[j] = __builtin_amdgcn_raw_buffer_load_b128(rw, voff, soff + j * S, LAUX);
    bool ok = true;
#pragma unroll
    for (int j = 0; j < N; ++j) ok &= (t[j][1] == t0) & (t[j][3] == t1);
    if (__all(ok) || budget <= 0) break;
    if (SLEEP) __builtin_amdgcn_s_sleep(SLEEP);
    if (calm) { __builtin_amdgcn_s_sleep(40); budget -= 40; }
    budget -= 16 + SLEEP;
    cbar();
  }
}

__global__ void __launch_bounds__(512, 2) fused_decode(Args a) {
  extern __shared__ __attribute__((aligned(16))) unsigned char lds[];
  LAS unsigned char* L = (LAS unsigned char*)lds;
  ctl_t ctl = (ctl_t)(L + L_CTL);
  const int tid = threadIdx.x, lane = tid & 63, wave = __builtin_amdgcn_readfirstlane(tid >> 6);
  const unsigned wg = (blockIdx.x & 7u) * 32u + (blockIdx.x >> 3);
  const unsigned g = wg >> 4, m = wg & 15u;
  const unsigned ks = m / 5u, cb = m - 5u * ks;
  const int g4 = lane >> 4, n16 = lane & 15;
  const unsigned t0 = now32();
  const unsigned org0 = t0 + ((START - (blockIdx.x >> 3) * SKEW) >> 8);
  int budget = 1 << 23;

  if (wave == 0) {
    ctl[lane] = 0u;
    wait_lds();
    __builtin_amdgcn_s_barrier();
    STAMP(0);
    __builtin_amdgcn_s_setprio(3);
    int voff[6];
#pragma unroll
    for (int j = 0; j < 6; ++j) {
      const int line = 8 * j + (lane >> 3), row = line / 3;
      voff[j] = line * 128 + (((lane & 7) ^ ((row >> 1) & 7)) << 4);
    }
    const __amdgpu_buffer_rsrc_t rk = __builtin_amdgcn_make_buffer_rsrc((void*)a.k, 0, (int)kKVBytes, 0x00020000);
    const __amdgpu_buffer_rsrc_t rv = __builtin_amdgcn_make_buffer_rsrc((void*)a.v, 0, (int)kKVBytes, 0x00020000);
    unsigned pub = 0, s = 0, sched = 0;
#pragma nounroll
    for (unsigned it = 0; it < 256u; ++it) {
      if (it >= NSLOT) {
        const unsigned q = it - NSLOT;
        if ((unsigned)__builtin_amdgcn_readfirstlane(ctl[C_DONE + (q & 3u)]) <= (q >> 2)) {
          STAMP(4);
          wait_vm<0>();
          pub = it; ctl[C_FULL] = pub;
          wait_ge<1>(ctl, C_DONE + (q & 3u), (q >> 2) + 1u, budget);
        }
        cbar();
      }
      wait_time(org0 + (sched >> 8));
      sched += PACE;
      const unsigned ph = (it & 127u) >> 4, sub = it & 15u;
      const unsigned so = ((ph * 16u + g) * 8192u + (sub * 16u + m) * 32u) * 384u;
      const __amdgpu_buffer_rsrc_t r = it >= 128u ? rv : rk;
#pragma unroll
      for (int j = 0; j < 12; ++j) {
        __builtin_amdgcn_raw_ptr_buffer_load_lds(r, (LAS void*)(L + s * SLOTB + j * 1024), 16, voff[j % 6], so + (j / 6) * 6144, 0, 2);
        wait_vm<INFL>();
        if (j == JP) {
          if (it + 1u > pub + LAG) { pub = it + 1u - LAG; ctl[C_FULL] = pub; }
        }
      }
      s = s + 1 == NSLOT ? 0 : s + 1;
    }
    STAMP(1);
    wait_vm<24>(); if (pub < 254u) ctl[C_FULL] = 254u;
    wait_vm<12>(); ctl[C_FULL] = 255u;
    wait_vm<0>(); ctl[C_FULL] = 256u;
    FLUSH();
    return;
  }

  const __amdgpu_buffer_rsrc_t rw = __builtin_amdgcn_make_buffer_rsrc((void*)a.ws, 0, (int)W_END, 0x00020000);
  unsigned tag0 = 0, tag1 = 0;

  {
    const bool fj = m >= 12u;
    const unsigned id = fj ? 192u + 3u * g + m - 12u : m < 6u ? 6u * g + m : 6u * (((g + 2u) & 15u) + 15u) + m;
    u64 ep = 0;
    if (wave >= 5) ep = __hip_atomic_load((GAS u64*)a.ws, RLX_AGENT);
    if (m < 15u && wave != 5) {
      if (wave != 7) {
        const unsigned u5 = wave == 6 ? 4u : (unsigned)wave - 1u;
        const unsigned stride = fj ? kF * 2 : kHD * 2, row0 = (id & 1u) * 320u + u5 * 64u;
        const __amdgpu_buffer_rsrc_t rs = __builtin_amdgcn_make_buffer_rsrc((void*)(fj ? a.w1 : a.wq), 0, kD * stride, 0x00020000);
        const unsigned voff = (unsigned)(32 * ((lane >> 3) & 1) + 8 * g4) * stride + (unsigned)(lane & 7) * 16u;
        const unsigned base = ((id >> 1) - (fj ? 96u : 0u)) * 128u + row0 * stride;
        v4u av = *(const GAS v4u*)((const GAS unsigned char*)a.x + (lane & 3) * 1280 + (row0 + 32 * ((lane >> 2) & 1) + 8 * g4) * 2);
        __builtin_amdgcn_s_barrier();
        v4u w[8];
#pragma unroll
        for (int j = 0; j < 8; ++j) w[j] = __builtin_amdgcn_raw_buffer_load_b128(rs, voff, base + j * stride, 2);
        if (lane & 8) av = v4u{0x3f803f80u, 0x3f803f80u, 0x3f803f80u, 0x3f803f80u};
        v4f acc[8];
#pragma unroll
        for (int d = 0; d < 4; ++d) {
          v4u b0, b1;
#pragma unroll
          for (int jj = 0; jj < 4; ++jj) {
            b0[jj] = __builtin_amdgcn_perm(w[2 * jj + 1][d], w[2 * jj][d], 0x05040100u);
            b1[jj] = __builtin_amdgcn_perm(w[2 * jj + 1][d], w[2 * jj][d], 0x07060302u);
          }
          acc[2 * d] = mfma(av, b0, v4f{0.f, 0.f, 0.f, 0.f});
          acc[2 * d + 1] = mfma(av, b1, v4f{0.f, 0.f, 0.f, 0.f});
        }
        if (g4 == ((lane >> 3) & 1)) {
          LAS unsigned char* p = L + L_PART + ((u5 * 2u + g4) * 64u + 8 * (lane & 7)) * 16u;
#pragma unroll
          for (int i = 0; i < 8; ++i) *(LAS v4f*)(p + i * 16) = acc[i];
        } else if (g4 == 2) {
          LAS unsigned char* p = L + L_CS + (u5 * 128u + n16 * 8) * 4u;
          *(LAS v4f*)p = v4f{acc[0][0], acc[1][0], acc[2][0], acc[3][0]};
          *(LAS v4f*)(p + 16) = v4f{acc[4][0], acc[5][0], acc[6][0], acc[7][0]};
        }
        wait_lds();
        ctl_add(L, C_PART, lane);
      } else {
        v4u xv[5];
#pragma unroll
        for (int j = 0; j < 5; ++j) xv[j] = *(const GAS v4u*)((const GAS unsigned char*)a.x + g4 * 1280 + (n16 + 16 * j) * 16);
        __builtin_amdgcn_s_barrier();
        float sum = 0.f;
#pragma unroll
        for (int j = 0; j < 5; ++j)
#pragma unroll
          for (int d = 0; d < 4; ++d) sum += lo_f(xv[j][d]) + hi_f(xv[j][d]);
        const float mean = row16_sum(sum) * (1.f / kD);
        float sq = 0.f;
#pragma unroll
        for (int j = 0; j < 5; ++j)
#pragma unroll
          for (int d = 0; d < 4; ++d) { const float d0 = lo_f(xv[j][d]) - mean, d1 = hi_f(xv[j][d]) - mean; sq += d0 * d0; sq += d1 * d1; }
        const float rstd = 1.f / sqrtf(row16_sum(sq) * (1.f / kD) + 1e-5f);
        STAMP(1);
        wait_ge<0>(ctl, C_PART, 5u, budget);
        v4f s = {0.f, 0.f, 0.f, 0.f};
        float cs = 0.f;
#pragma unroll
        for (int i = 0; i < 10; ++i) { s += *(const LAS v4f*)(L + L_PART + (i * 64 + lane) * 16); cs += *(const LAS float*)(L + L_CS + (i * 64 + lane) * 4); }
        float r[4];
#pragma unroll
        for (int b = 0; b < 4; ++b) r[b] = (s[b] - rdlane(mean, 16 * b) * cs) * rdlane(rstd, 16 * b);
        put(rw, lane * 16, W_QX + id * 1024u, pk_bf16(r[0], r[1]), pk_bf16(r[2], r[3]), (unsigned)ep + 1u, (unsigned)(ep >> 32) + 1u);
        STAMP(2);
      }
    } else {
      __builtin_amdgcn_s_barrier();
    }
    if (wave >= 5) {
      tag0 = __builtin_amdgcn_readfirstlane((unsigned)ep) + 1u;
      tag1 = __builtin_amdgcn_readfirstlane((unsigned)(ep >> 32)) + 1u;
      STAMP(0);
      const unsigned kind = wave - 5;
      if (kind < 2u) {
        wait_time(t0 + (unsigned)(POLL_US * 100.0) + kind * 150u);
        v4u t[6];
        sweep<6, 8>(rw, lane * 16, W_QX + (kind * 16u + g) * 6144u, 1024u, tag0, tag1, t, budget);
#pragma unroll
        for (int j = 0; j < 6; ++j) {
          const unsigned u0 = t[j & 6][2 * (j & 1)], u1 = t[j | 1][2 * (j & 1)];
          const unsigned pk = pk_bf16(lo_f(u0) + lo_f(u1), hi_f(u0) + hi_f(u1));
          LAS unsigned char* q = L + L_QF + (kind * 4u + 2 * (j & 1)) * 384u + (64 * (j >> 1) + lane) * 2;
          st16(q, pk);
          st16(q + 384, pk >> 16);
        }
        wait_lds();
        ctl[C_Q + kind] = 1u;
      }
      if (kind == 1u && m < 15u) {
        const unsigned col = 96u * g + 32u * ks;
        const float bias = bf2f(a.b1[col + (lane & 31)]);
        v4u t[1];
        sweep<1, 8>(rw, (lane >> 5) * 1024 + (lane & 31) * 16, W_QX + (96u + (col >> 6)) * 2048u + (col & 63u) * 16u, 0u, tag0, tag1, t, budget);
        const float r0 = fmaxf(swap32_sum(lo_f(t[0][0]), lo_f(t[0][2])) + bias, 0.f), r1 = fmaxf(swap32_sum(hi_f(t[0][0]), hi_f(t[0][2])) + bias, 0.f);
        const unsigned pk = pk_bf16(r0 * r0, r1 * r1);
        LAS unsigned char* o = L + L_OA + (lane >> 5) * 640 + 256 + (lane & 31) * 2;
        st16(o, pk);
        st16(o + 320, pk >> 16);
        wait_lds();
      }
      ctl[C_TAG] = tag0; ctl[C_TAG + 1] = tag1;
      wait_lds();
      ctl_add(L, C_PRO, lane);
      STAMP(3);
    }
  }

  if (wave <= 4) {
    const int c = wave - 1;
    unsigned s = c;
    STAMP(1);
    {
      const unsigned ka0 = n16 * 384 + ((g4 ^ (n16 >> 1)) << 4), ka1 = ka0 ^ 64u;
#pragma nounroll
      for (unsigned ph = 0; ph < 8u; ++ph) {
        v4u kf[4][12];
#pragma unroll
        for (int ii = 0; ii < 4; ++ii) {
          wait_ge<2>(ctl, C_FULL, ph * 16u + ii * 4u + c + 1u, budget);
          LAS unsigned char* p0 = L + s * SLOTB + ka0;
          LAS unsigned char* p1 = L + s * SLOTB + ka1;
#pragma unroll
          for (int t = 0; t < 6; ++t)
#pragma unroll
            for (int hf = 0; hf < 2; ++hf) kf[ii][hf * 6 + t] = *(const LAS v4u*)(((t & 1) ? p1 : p0) + hf * 6144 + (t >> 1) * 128);
          wait_lds();
          ctl[C_DONE + c] = ph * 4u + ii + 1u;
          s += 4; if (s >= NSLOT) s -= NSLOT;
        }
        if (ph < 3u) wait_ge<1>(ctl, ph < 2u ? C_Q + ph : C_PRO, ph < 2u ? 1u : 3u, budget);
        if (ph == 0u) STAMP(2);
        if (ph == 1u) STAMP(3);
        v4u qf[6];
#pragma unroll
        for (int t = 0; t < 6; ++t) qf[t] = *(const LAS v4u*)(L + L_QF + ((ph & 1u) * 4u + (ph >> 1)) * 384u + t * 64 + g4 * 16);
#pragma unroll
        for (int ii = 0; ii < 4; ii += 2) {
          v4f d[4];
#pragma unroll
          for (int i = 0; i < 4; ++i) d[i] = v4f{0.f, 0.f, 0.f, 0.f};
#pragma unroll
          for (int t = 0; t < 6; ++t)
#pragma unroll
            for (int i = 0; i < 4; ++i) d[i] = mfma(qf[t], kf[ii + (i >> 1)][(i & 1) * 6 + t], d[i]);
          const float sc = sel(g4 & 2, sel(g4 & 1, d[3][0], d[2][0]), sel(g4 & 1, d[1][0], d[0][0]));
          *(LAS float*)(L + L_SC + ph * 2048u + c * 128 + (ii + (lane >> 5)) * 512 + (lane & 31) * 4) = sc;
        }
        wait_lds();
        ctl[C_S + c] = ph + 1u;
      }
    }
    tag0 = ctl[C_TAG]; tag1 = ctl[C_TAG + 1];
    {
      const int q = n16 >> 2, p = lane & 3;
      unsigned va[2];
#pragma unroll
      for (int t = 0; t < 2; ++t) {
        const int row = 8 * g4 + (q & 1) + 4 * (q >> 1) + 2 * t;
        va[t] = row * 384 + ((((p >> 1)) ^ ((row >> 1) & 7)) << 4) + 8 * (p & 1);
      }
#pragma nounroll
      for (unsigned ph = 0; ph < 8u; ++ph) {
        wait_ge<1>(ctl, C_P, ph + 1u, budget);
        v4f acc[12];
#pragma unroll
        for (int i = 0; i < 12; ++i) acc[i] = v4f{0.f, 0.f, 0.f, 0.f};
#pragma nounroll
        for (unsigned ii = 0; ii < 4u; ++ii) {
          const unsigned sub = ii * 4u + c, it = 128u + ph * 16u + sub;
          const v4u pa = *(const LAS v4u*)(L + L_SC + ph * 2048u + (lane & 1) * 1024 + sub * 64u + g4 * 16);
          wait_ge<1>(ctl, C_FULL, it + 1u, budget);
          const unsigned b0 = s * SLOTB + va[0], b1 = s * SLOTB + va[1];
          v4u vf[12];
#pragma unroll
          for (int t = 0; t < 12; ++t) vf[t] = tr_read2(L + (b0 ^ ((t & 3) << 5)) + (t >> 2) * 128, L + (b1 ^ ((t & 3) << 5)) + (t >> 2) * 128);
          __builtin_amdgcn_sched_barrier(0);
#pragma unroll
          for (int t = 0; t < 12; ++t) acc[t] = mfma(pa, vf[t], acc[t]);
          wait_lds();
          ctl[C_DONE + c] = (it >> 2) + 1u;
          s += 4; if (s >= NSLOT) s -= NSLOT;
        }
        v4f o;
#pragma unroll
        for (int i = 0; i < 4; ++i)
          o[i] = sel(g4 & 2, acc[8 + i][0], sel(g4 & 1, acc[4 + i][0], acc[i][0])) + sel(g4 & 2, acc[8 + i][1], sel(g4 & 1, acc[4 + i][1], acc[i][1]));
        LAS unsigned char* ob = L + L_OPB + (ph & 1u) * 2304u + (lane < 48 ? lane : lane - 16) * 16;
        if (c < 3) {
          if (ph >= 2u) wait_ge<1>(ctl, C_OFREE, ph - 1u, budget);
          *(LAS v4f*)(ob + c * 768) = o;
          wait_lds();
          ctl[C_ODONE + c] = ph + 1u;
        } else {
          if (ph == 7u) STAMP(4);
          wait_ge<0>(ctl, C_ODONE + 0, ph + 1u, budget); wait_ge<0>(ctl, C_ODONE + 1, ph + 1u, budget); wait_ge<0>(ctl, C_ODONE + 2, ph + 1u, budget);
          o = ((*(const LAS v4f*)ob + *(const LAS v4f*)(ob + 768)) + *(const LAS v4f*)(ob + 1536)) + o;
          v2u pk = {pk_bf16(o[0], o[1]), pk_bf16(o[2], o[3])};
          if (lane < 48) {
            if (ph < PUB_PH) *(LAS v2u*)(L + L_ST + ph * 384u + lane * 8) = pk;
            else put(rw, lane * 16, W_H1 + (wg * 8u + ph) * 768u, pk[0], pk[1], tag0, tag1);
            if (ph == PUB_PH) {
#pragma nounroll
              for (unsigned i = 0; i < PUB_PH; ++i) {
                pk = *(const LAS v2u*)(L + L_ST + i * 384u + lane * 8);
                put(rw, lane * 16, W_H1 + (wg * 8u + i) * 768u, pk[0], pk[1], tag0, tag1);
              }
            }
          }
          ctl[C_OFREE] = ph + 1u;
        }
      }
    }
    if (m < 15u) {
      if (c < 3) STAMP(4);
      wait_ge<1>(ctl, C_G, 7u, budget);
      const int q = n16 >> 2;
      const int bo = (8 * g4 + q) * 256 + ((c * 64) ^ ((q | (g4 & 1) << 2) << 5)) + (lane & 3) * 8;
      LAS unsigned char* ba = L + L_TAIL + bo;
      LAS unsigned char* bb = L + L_TAIL + (bo ^ 32);
      LAS unsigned char* aa = L + L_OA + (lane & 3) * 320 + g4 * 16;
      const v4u a0 = *(const LAS v4u*)aa, a1 = *(const LAS v4u*)(aa + 64), a4 = *(const LAS v4u*)(aa + 256);
      wait_ge<0>(ctl, C_WO, 1u, budget);
      v4u f0[5], f1[5];
#pragma unroll
      for (int t = 0; t < 5; ++t) { f0[t] = tr_read2(ba + t * 8192, ba + t * 8192 + 1024); f1[t] = tr_read2(bb + t * 8192, bb + t * 8192 + 1024); }
      v4f y0 = mfma(a0, f0[0], v4f{0.f, 0.f, 0.f, 0.f}), y1 = mfma(a0, f1[0], v4f{0.f, 0.f, 0.f, 0.f});
      y0 = mfma(a1, f0[1], y0); y1 = mfma(a1, f1[1], y1);
      y0 = mfma(a4, f0[4], y0); y1 = mfma(a4, f1[4], y1);
      asm volatile("" : "+v"(y0), "+v"(y1), "+v"(f0[2]), "+v"(f0[3]), "+v"(f1[2]), "+v"(f1[3]) :: "memory");
      STAMP(5);
      wait_ge<0>(ctl, C_G, 8u, budget);
      STAMP(6);
      const v4u a2 = *(const LAS v4u*)(aa + 128), a3 = *(const LAS v4u*)(aa + 192);
      y0 = mfma(a2, f0[2], y0); y1 = mfma(a2, f1[2], y1);
      y0 = mfma(a3, f0[3], y0); y1 = mfma(a3, f1[3], y1);
      const float z0 = sel(g4 & 2, sel(g4 & 1, y1[2], y1[0]), sel(g4 & 1, y0[2], y0[0]));
      const float z1 = sel(g4 & 2, sel(g4 & 1, y1[3], y1[1]), sel(g4 & 1, y0[3], y0[1]));
      put(rw, (lane >> 3) * 6144 + (lane & 7) * 16, W_H2 + (cb * 32u + c * 8u) * 6144u + (g * 3u + ks) * 128u, asu(z0), asu(z1), tag0, tag1);
    }
    FLUSH();
    return;
  }

  if (wave == 7) {
    if (m < 15u) {
#pragma nounroll
      for (unsigned q = 256u - NSLOT; q < 260u - NSLOT; ++q) wait_ge<8>(ctl, C_DONE + (q & 3u), (q >> 2) + 1u, budget);
      wait_ge<1>(ctl, C_FULL, TAIL_IT, budget);
      STAMP(4);
      const __amdgpu_buffer_rsrc_t ro = __builtin_amdgcn_make_buffer_rsrc((void*)a.wo, 0, kHD * kD * 2, 0x00020000);
      const __amdgpu_buffer_rsrc_t r2 = __builtin_amdgcn_make_buffer_rsrc((void*)a.w2, 0, kF * kD * 2, 0x00020000);
      const int ch = (n16 ^ (2 * g4)) * 16;
#pragma nounroll
      for (unsigned p = 0; p < 32u; p += 4u) {
        const unsigned so = (g * 192u + ks * 64u + (p >> 4) * 3072u + (p & 15u)) * 1280u + cb * 256u;
#pragma unroll
        for (int j = 0; j < 4; ++j)
          __builtin_amdgcn_raw_ptr_buffer_load_lds(ro, (LAS void*)(L + L_TAIL + (p + j) * 1024), 16, g4 * 20480 + (ch ^ ((j & 2) << 6)), so + j * 1280, 0, 2);
      }
#pragma unroll
      for (int j = 0; j < 8; ++j)
        __builtin_amdgcn_raw_ptr_buffer_load_lds(r2, (LAS void*)(L + L_TAIL + (32 + j) * 1024), 16, g4 * 1280 + (ch ^ ((j & 2) << 6)), (g * 96u + ks * 32u + 4u * j) * 1280u + cb * 256u, 0, 2);
      STAMP(5);
      wait_vm<0>();
      ctl[C_WO] = 1u;
    }
    FLUSH();
    return;
  }
  if (wave == 6) {
    if (m < 15u) {
      wait_ge<8>(ctl, C_FULL, 128u + 16u * PUB_PH + GDELAY, budget);
      {
        v4u t[2];
        sweep<2, 0>(rw, n16 * 128 + g4 * 16, W_ML + g * 2048u, 64u, tag0, tag1, t, budget, true);
#pragma unroll
        for (int h = 0; h < 2; ++h) {
          const float mj = asf(t[h][0]), lj = asf(t[h][2]);
          const float wj = __builtin_amdgcn_exp2f((mj - row16_max(mj)) * kC);
          *(LAS float*)(L + L_WT + h * 256 + lane * 4) = wj / row16_sum(wj * lj);
        }
      }
      cbar();
      STAMP(1);
#pragma nounroll
      for (unsigned ph = 0; ph < 8u; ++ph) {
        const v4f wt = *(const LAS v4f*)(L + L_WT + ph * 64u + g4 * 16);
        if (ph < 7u) wait_ge<8>(ctl, C_FULL, 128u + 16u * ph + GDELAY, budget);
        else { STAMP(2); wait_ge<1>(ctl, C_FULL, 256u, budget); STAMP(4); }
        v4u t[4];
        sweep<4, 0>(rw, g4 * 24576 + n16 * 16, W_H1 + (g * 128u + ph) * 768u + ks * 256u, 6144u, tag0, tag1, t, budget, ph < 7u);
        float v[4];
#pragma unroll
        for (int i = 0; i < 2; ++i) {
          v[2 * i] = (lo_f(t[0][2 * i]) * wt[0] + lo_f(t[1][2 * i]) * wt[1]) + (lo_f(t[2][2 * i]) * wt[2] + lo_f(t[3][2 * i]) * wt[3]);
          v[2 * i + 1] = (hi_f(t[0][2 * i]) * wt[0] + hi_f(t[1][2 * i]) * wt[1]) + (hi_f(t[2][2 * i]) * wt[2] + hi_f(t[3][2 * i]) * wt[3]);
        }
        const float o = swap16_sum(swap32_sum(v[0], v[2]), swap32_sum(v[1], v[3]));
        st16(L + L_OA + (ph >> 1) * 320u + (ph & 1u) * 128u + n16 * 8 + g4 * 2, pk_bf16(o, 0.f));
        wait_lds();
        ctl[C_G] = ph + 1u;
      }
    }
    FLUSH();
    return;
  }
  {
    const unsigned j = g * 10u + (m < 10u ? m : 0u);
    const unsigned col = (j >> 3) * 32u + (j & 4u) * 4u + (j & 1u) * 8u + (lane & 7);
    const unsigned oidx = ((j & 2u) + (lane >> 5)) * kD + col;
    const float resid = bf2f(a.x[oidx]) + bf2f(a.b2[col]);
    wait_ge<1>(ctl, C_PRO, 3u, budget);
    float mxs = 0.f, ls = 0.f;
#pragma nounroll
    for (unsigned ph = 0; ph < 8u; ++ph) {
#pragma nounroll
      for (int c = 0; c < 4; ++c) wait_ge<4>(ctl, C_S + c, ph + 1u, budget);
      LAS unsigned char* sp = L + L_SC + ph * 2048u;
      const v4f s0 = *(const LAS v4f*)(sp + lane * 32), s1 = *(const LAS v4f*)(sp + lane * 32 + 16);
      float mx = fmaxf(fmaxf(fmaxf(s0[0], s0[1]), fmaxf(s0[2], s0[3])), fmaxf(fmaxf(s1[0], s1[1]), fmaxf(s1[2], s1[3])));
      mx = row16_max(mx);
      mx = fmaxf(fmaxf(rdlane(mx, 0), rdlane(mx, 16)), fmaxf(rdlane(mx, 32), rdlane(mx, 48)));
      float p[8];
#pragma unroll
      for (int i = 0; i < 4; ++i) { p[i] = __builtin_amdgcn_exp2f((s0[i] - mx) * kC); p[4 + i] = __builtin_amdgcn_exp2f((s1[i] - mx) * kC); }
      float l = ((p[0] + p[1]) + (p[2] + p[3])) + ((p[4] + p[5]) + (p[6] + p[7]));
      l = row16_sum(l);
      l = (rdlane(l, 0) + rdlane(l, 16)) + (rdlane(l, 32) + rdlane(l, 48));
      v4u hi, lo;
#pragma unroll
      for (int d = 0; d < 4; ++d) {
        const int i = (d == 1) ? 4 : (d == 2) ? 2 : 2 * d;
        hi[d] = pk_bf16(p[i], p[i + 1]);
        lo[d] = pk_bf16(p[i] - lo_f(hi[d]), p[i + 1] - hi_f(hi[d]));
      }
      cbar();
      *(LAS v4u*)(sp + lane * 16) = hi;
      *(LAS v4u*)(sp + 1024 + lane * 16) = lo;
      if ((unsigned)lane == ph) { mxs = mx; ls = l; }
      wait_lds();
      ctl[C_P] = ph + 1u;
    }
    STAMP(1);
    wait_ge<8>(ctl, C_FULL, 144u + 16u * PUB_PH, budget);
    if (lane < 8) put(rw, lane * 16, W_ML + wg * 128u, asu(mxs), asu(ls), tag0, tag1);
    STAMP(2);
    if (m < 10u) {
      wait_ge<8>(ctl, C_FULL, 250u, budget);
      wait_ge<1>(ctl, C_G, 8u, budget);
      STAMP(5);
      v4u t[6];
      sweep<6, 0>(rw, lane * 16, W_H2 + j * 6144u, 1024u, tag0, tag1, t, budget);
      STAMP(6);
      const float y0 = ((asf(t[0][0]) + asf(t[1][0])) + (asf(t[2][0]) + asf(t[3][0]))) + (asf(t[4][0]) + asf(t[5][0]));
      const float y1 = ((asf(t[0][2]) + asf(t[1][2])) + (asf(t[2][2]) + asf(t[3][2]))) + (asf(t[4][2]) + asf(t[5][2]));
      float y = swap32_sum(y0, y1);
      y = swap16_sum(y, y);
      y += dpp<0x128>(y);
      if ((lane & 24) == 0) a.out[oidx] = (unsigned short)pk_bf16(y + resid, 0.f);
      if (wg == 0u && lane == 0) __hip_atomic_store((GAS u64*)a.ws, ((u64)tag1 << 32) | tag0, RLX_AGENT);
    }
  }
  FLUSH();
}

}

extern "C" void kernel_launch(void* const* d_in, const int* in_sizes, int n_in,
                              void* d_out, int out_size, void* d_ws, size_t ws_size,
                              hipStream_t stream) {
  (void)in_sizes; (void)n_in; (void)out_size; (void)ws_size;
  Args a;
  a.x = (const unsigned short*)d_in[0];
  a.k = (const unsigned short*)d_in[1];
  a.v = (const unsigned short*)d_in[2];
  a.wq = (const unsigned short*)d_in[3];
  a.wo = (const unsigned short*)d_in[4];
  a.w1 = (const unsigned short*)d_in[6];
  a.b1 = (const unsigned short*)d_in[7];
  a.w2 = (const unsigned short*)d_in[8];
  a.b2 = (const unsigned short*)d_in[9];
  a.out = (unsigned short*)d_out;
  a.ws = (unsigned char*)d_ws;
  (void)hipFuncSetAttribute((const void*)fused_decode, hipFuncAttributeMaxDynamicSharedMemorySize, L_BYTES);
  fused_decode<<<256, 512, L_BYTES, stream>>>(a);
}
```

```cpp
#include <hip/hip_runtime.h>
#include <hip/hip_bf16.h>
#include <stdint.h>

typedef unsigned v4u __attribute__((ext_vector_type(4)));
typedef unsigned v2u __attribute__((ext_vector_type(2)));
typedef unsigned v16u __attribute__((ext_vector_type(16)));
typedef float v4f __attribute__((ext_vector_type(4)));
typedef short v4s __attribute__((ext_vector_type(4)));
typedef __bf16 v8bf __attribute__((ext_vector_type(8)));
typedef __bf16 v2bf __attribute__((ext_vector_type(2)));
typedef float v2f __attribute__((ext_vector_type(2)));
typedef unsigned long long u64;
#define LAS __attribute__((address_space(3)))
#define GAS __attribute__((address_space(1)))
#define RLX_AGENT __ATOMIC_RELAXED, __HIP_MEMORY_SCOPE_AGENT

#define PACE_TBS 7.9
#define START_US 0.5
#define SKEW_US 0.45
#define GAP_IT 4u
#define GAP_US 1.9
#define STAG_US 0.26
#define POLL_US 6.0
#define NINFL 32
#define PUB_IT 240u
#define ML_IT 247u
#define H0_IT 251u
#define TAIL_IT 253
#define DLY1 30
#define DLY2 22
#define FB_N 14u
#define FB_0 9.1
#define FB_P 6.4
#define FB_D 2.2
#define FB_THR 0.6
#define FB_G 1.25f
#define FB_RAMP 0.08
#define FB_RAMP2 0.1
#define FB_IT2 228u
#define HOLD_J 1.0
#define HOLD_X 0.2
#define HOLD_N 24u
#define CODE_PF 145
#define AUX_DEV 16
#define AUX_NT 2

namespace {

constexpr int kD = 640, kHD = 6144, kF = 1536;
constexpr unsigned kKVBytes = 4u * 32u * 8192u * 384u;
constexpr int SLOTB = 12288, NSLOT = 11, INFL = NINFL;
constexpr int LAG = (INFL + 11) / 12, JP = INFL + 11 - 12 * LAG;
constexpr unsigned PACE = (unsigned)(12288.0 * 256.0 / (PACE_TBS * 1e12) * 1e8 * 256.0);
constexpr unsigned START = (unsigned)(START_US * 100.0 * 256.0);
constexpr unsigned GAP = (unsigned)(GAP_US * 100.0 * 256.0), STAG = (unsigned)(STAG_US * 100.0);
constexpr unsigned SKEW = (unsigned)(SKEW_US * 100.0 * 256.0 / 31.0);
constexpr unsigned RAMP = (unsigned)(PACE * FB_RAMP), RAMP2 = (unsigned)(PACE * FB_RAMP2);
constexpr int TAIL_SLOT = 256 % NSLOT;
typedef char compile_check_1[(TAIL_SLOT + 4 <= NSLOT) ? 1 : -1];
typedef char compile_check_2[(INFL >= 24 && INFL <= 36) ? 1 : -1];
typedef char compile_check_3[(START >= 31 * SKEW) ? 1 : -1];

constexpr int L_SC = NSLOT * SLOTB;
constexpr int L_QF = L_SC + 16384;
constexpr int L_OPB = L_QF + 3072;
constexpr int L_OA = L_OPB + 4608;
constexpr int L_CS = L_OA + 1280;
constexpr int L_CTL = L_CS + 2560;
constexpr int L_BYTES = L_CTL + 512;
typedef char compile_check_4[(L_BYTES <= 163840) ? 1 : -1];
constexpr int L_ST = L_QF;
constexpr int L_WT = L_CS;
constexpr int L_PART = L_SC + 4096;
constexpr int L_TAIL = TAIL_SLOT * SLOTB;
enum { C_FULL = 0, C_PART = 1, C_PRO = 2, C_P = 3, C_DONE = 4  , C_ODONE = 8  , C_OFREE = 11, C_Q = 12  ,
       C_WO = 15, C_G = 16, C_TAG = 18  , C_S = 20  , C_LATE = 24, C_SH = 26   };

constexpr unsigned W_QX = 256;
constexpr unsigned W_ML = W_QX + 240 * 1024;
constexpr unsigned W_H0 = W_ML + 256 * 128;
constexpr unsigned W_H1 = W_H0 + 256 * 3072;
constexpr unsigned W_H2 = W_H1 + 256 * 768;
constexpr unsigned W_FB = W_H2 + 160 * 6144;
constexpr unsigned W_END = W_FB + 32 * 1024;

constexpr float kC = 0.07216878364870322f * 1.4426950408889634f;

struct Args {
  const unsigned short *x, *k, *v, *wq, *wo, *w1, *b1, *w2, *b2;
  unsigned short* out;
  unsigned char* ws;
};

template <int N> __device__ __forceinline__ void wait_vm() { asm volatile("s_waitcnt vmcnt(%0)" ::"n"(N) : "memory"); }
__device__ __forceinline__ void wait_lds() { asm volatile("s_waitcnt lgkmcnt(0)" ::: "memory"); }
__device__ __forceinline__ void cbar() { asm volatile("" ::: "memory"); }
__device__ __forceinline__ unsigned now32() { return (unsigned)__builtin_amdgcn_s_memrealtime(); }
__device__ __forceinline__ float asf(unsigned u) { return __builtin_bit_cast(float, u); }
__device__ __forceinline__ unsigned asu(float f) { return __builtin_bit_cast(unsigned, f); }
__device__ __forceinline__ float bf2f(unsigned short h) { return asf((unsigned)h << 16); }
__device__ __forceinline__ unsigned pk_bf16(float a, float b) {
  v2f f = {a, b};
  return __builtin_bit_cast(unsigned, __builtin_convertvector(f, v2bf));
}
__device__ __forceinline__ float lo_f(unsigned u) { return asf(u << 16); }
__device__ __forceinline__ float hi_f(unsigned u) { return asf(u & 0xffff0000u); }
__device__ __forceinline__ v4f mfma(v4u a, v4u b, v4f c) {
  return __builtin_amdgcn_mfma_f32_16x16x32_bf16(__builtin_bit_cast(v8bf, a), __builtin_bit_cast(v8bf, b), c, 0, 0, 0);
}
__device__ __forceinline__ v4u tr_read2(LAS unsigned char* p0, LAS unsigned char* p1) {
  const v2u lo = __builtin_bit_cast(v2u, __builtin_amdgcn_ds_read_tr16_b64_v4i16((LAS v4s*)p0));
  const v2u hi = __builtin_bit_cast(v2u, __builtin_amdgcn_ds_read_tr16_b64_v4i16((LAS v4s*)p1));
  return v4u{lo[0], lo[1], hi[0], hi[1]};
}
template <int CTRL> __device__ __forceinline__ float dpp(float v) {
  return asf(__builtin_amdgcn_update_dpp(0, (int)asu(v), CTRL, 0xf, 0xf, true));
}
__device__ __forceinline__ float row16_sum(float v) {
  v += dpp<0x128>(v); v += dpp<0x124>(v); v += dpp<0x122>(v); v += dpp<0x121>(v);
  return v;
}
__device__ __forceinline__ float row16_max(float v) {
  v = fmaxf(v, dpp<0x128>(v)); v = fmaxf(v, dpp<0x124>(v)); v = fmaxf(v, dpp<0x122>(v)); v = fmaxf(v, dpp<0x121>(v));
  return v;
}
__device__ __forceinline__ float sel(bool c, float a, float b) { return c ? a : b; }
__device__ __forceinline__ float rdlane(float v, int l) { return asf(__builtin_amdgcn_readlane(asu(v), l)); }
__device__ __forceinline__ float swap32_sum(float a, float b) {
  const v2u r = __builtin_amdgcn_permlane32_swap(asu(a), asu(b), false, false);
  return asf(r[0]) + asf(r[1]);
}
__device__ __forceinline__ float swap16_sum(float a, float b) {
  const v2u r = __builtin_amdgcn_permlane16_swap(asu(a), asu(b), false, false);
  return asf(r[0]) + asf(r[1]);
}
__device__ __forceinline__ void st16(LAS unsigned char* p, unsigned v) { *(LAS unsigned short*)p = (unsigned short)v; }

typedef volatile LAS unsigned* ctl_t;

template <int SLEEP> __device__ __forceinline__ void wait_ge(ctl_t ctl, int idx, unsigned need, int& budget) {
#pragma nounroll
  while ((unsigned)__builtin_amdgcn_readfirstlane(ctl[idx]) < need && budget > 0) {
    if (SLEEP) __builtin_amdgcn_s_sleep(SLEEP);
    budget -= 1 + SLEEP;
  }
  cbar();
}
__device__ __forceinline__ void wait_time(unsigned due) {
#pragma nounroll
  while ((int)(now32() - due) < 0) __builtin_amdgcn_s_sleep(1);
}
__device__ __forceinline__ void ctl_add(LAS unsigned char* L, int idx, int lane) {
  if (lane == 0) __hip_atomic_fetch_add((LAS unsigned*)(L + L_CTL) + idx, 1u, __ATOMIC_RELAXED, __HIP_MEMORY_SCOPE_WORKGROUP);
}
__device__ __forceinline__ void put(__amdgpu_buffer_rsrc_t rw, unsigned voff, unsigned soff, unsigned v0, unsigned v1, unsigned t0, unsigned t1) {
  __builtin_amdgcn_raw_buffer_store_b128(v4u{v0, t0, v1, t1}, rw, voff, soff, AUX_DEV);
}
template <int N, int SLEEP>
__device__ __forceinline__ void sweep(__amdgpu_buffer_rsrc_t rw, unsigned voff, unsigned soff, unsigned S, unsigned t0, unsigned t1, v4u (&t)[N], int& budget) {
#pragma nounroll
  for (;;) {
#pragma unroll
    for (int j = 0; j < N; ++j) t[j] = __builtin_amdgcn_raw_buffer_load_b128(rw, voff, soff + j * S, AUX_DEV);
    bool ok = true;
#pragma unroll
    for (int j = 0; j < N; ++j) ok &= (t[j][1] == t0) & (t[j][3] == t1);
    if (__all(ok) || budget <= 0) break;
    if (SLEEP) __builtin_amdgcn_s_sleep(SLEEP);
    budget -= 16 + SLEEP;
    cbar();
  }
}

__device__ __forceinline__ const unsigned short* mkp(unsigned lo, unsigned hi) { return (const unsigned short*)(((u64)hi << 32) | lo); }

__global__ void __launch_bounds__(512, 2) fused_decode(Args) {
  v16u ka; v4u kb; v2u kc, tt;
  asm volatile("s_memrealtime %3\n\ts_load_dwordx16 %0, %4, 0x0\n\ts_load_dwordx4 %1, %4, 0x40\n\ts_load_dwordx2 %2, %4, 0x50"
               : "=&s"(ka), "=&s"(kb), "=&s"(kc), "=&s"(tt) : "s"(__builtin_amdgcn_kernarg_segment_ptr()));
  extern __shared__ __attribute__((aligned(16))) unsigned char lds[];
  LAS unsigned char* L = (LAS unsigned char*)lds;
  ctl_t ctl = (ctl_t)(L + L_CTL);
  const int tid = threadIdx.x, lane = tid & 63, wave = __builtin_amdgcn_readfirstlane(tid >> 6);
  const unsigned wg = (blockIdx.x & 7u) * 32u + (blockIdx.x >> 3);
  const unsigned g = wg >> 4, m = wg & 15u;
  const unsigned ks = m / 5u, cb = m - 5u * ks;
  const int g4 = lane >> 4, n16 = lane & 15;
  unsigned pf = 0;
  if (wave >= 5) pf = *(const GAS unsigned*)((__builtin_amdgcn_s_getpc() & ~255ull) + min(511 - tid, CODE_PF - 1) * 128u);
  if (wave == 0) ctl[lane] = 0u;
  asm volatile("s_waitcnt lgkmcnt(0)\n\ts_barrier" : "+s"(ka), "+s"(kb), "+s"(kc), "+s"(tt) :: "memory");
  Args a;
  a.x = mkp(ka[0], ka[1]); a.k = mkp(ka[2], ka[3]); a.v = mkp(ka[4], ka[5]); a.wq = mkp(ka[6], ka[7]); a.wo = mkp(ka[8], ka[9]);
  a.w1 = mkp(ka[10], ka[11]); a.b1 = mkp(ka[12], ka[13]); a.w2 = mkp(ka[14], ka[15]); a.b2 = mkp(kb[0], kb[1]);
  a.out = (unsigned short*)mkp(kb[2], kb[3]); a.ws = (unsigned char*)mkp(kc[0], kc[1]);
  const unsigned t0 = tt[0];
  const unsigned org0 = t0 + ((START - (blockIdx.x >> 3) * SKEW) >> 8);
  int budget = 1 << 23;
  if (__builtin_expect(wave == 0, 1)) {
    __builtin_amdgcn_s_setprio(3);
    int voff[6];
#pragma unroll
    for (int j = 0; j < 6; ++j) {
      const int line = 8 * j + (lane >> 3), row = line / 3;
      voff[j] = line * 128 + (((lane & 7) ^ ((row >> 1) & 7)) << 4);
    }
    const __amdgpu_buffer_rsrc_t rk = __builtin_amdgcn_make_buffer_rsrc((void*)a.k, 0, (int)kKVBytes, 0x00020000);
    const __amdgpu_buffer_rsrc_t rv = __builtin_amdgcn_make_buffer_rsrc((void*)a.v, 0, (int)kKVBytes, 0x00020000);
    unsigned pub = 0, s = 0, sched = 0, sh = 0, hold = 0, hold_end = 0;
    int prev = 0;
#pragma nounroll
    for (unsigned it = 0; it < 256u; ++it) {
      const unsigned q = it - NSLOT;
      const unsigned dn = ctl[C_DONE + (q & 3u)];
      const v2u f = *(volatile LAS v2u*)(L + L_CTL + C_SH * 4);
      unsigned t = now32();
      if (it >= NSLOT && (unsigned)__builtin_amdgcn_readfirstlane(dn) <= (q >> 2)) {
        wait_vm<0>();
        pub = it; ctl[C_FULL] = pub;
        wait_ge<1>(ctl, C_DONE + (q & 3u), (q >> 2) + 1u, budget);
      }
      cbar();
      if (it == GAP_IT) sched += GAP;
      const unsigned fl = __builtin_amdgcn_readfirstlane(f[1]), base = org0 + (sched >> 8);
      if (it == hold_end || fl > sh + 12800u) hold = 0;
      sh = min(max(sh + (it >= FB_IT2 ? RAMP2 : RAMP), fl), (unsigned)__builtin_amdgcn_readfirstlane(f[0]));
      if ((int)(t - base) - prev > (int)(HOLD_J * 100.0) && prev - (int)(sh >> 8) < 30 && it > 24u && m != 1u) {
        hold = (t - base - (int)(HOLD_X * 100.0)) << 8;
        hold_end = it + HOLD_N;
      }
      const unsigned due = org0 + ((sched + max(sh, hold)) >> 8);
#pragma nounroll
      while ((int)(t - due) < 0) { __builtin_amdgcn_s_sleep(1); t = now32(); }
      prev = (int)(t - base);
      ctl[C_LATE] = prev;
      const unsigned ph = (it & 127u) >> 4, sub = it & 15u;
      sched += PACE;
      const unsigned so = ((ph * 16u + g) * 8192u + (sub * 16u + m) * 32u) * 384u;
      const __amdgpu_buffer_rsrc_t r = it >= 128u ? rv : rk;
#pragma unroll
      for (int j = 0; j < 12; ++j) {
        __builtin_amdgcn_raw_ptr_buffer_load_lds(r, (LAS void*)(L + s * SLOTB + j * 1024), 16, voff[j % 6], so + (j / 6) * 6144, 0, AUX_NT);
        wait_vm<INFL>();
        if (j == JP) {
          if (it + 1u > pub + LAG) { pub = it + 1u - LAG; ctl[C_FULL] = pub; }
        }
      }
      s = s + 1 == NSLOT ? 0 : s + 1;
    }
    wait_vm<24>(); if (pub < 254u) ctl[C_FULL] = 254u;
    wait_vm<12>(); ctl[C_FULL] = 255u;
    wait_vm<0>(); ctl[C_FULL] = 256u;
    return;
  }

  const __amdgpu_buffer_rsrc_t rw = __builtin_amdgcn_make_buffer_rsrc((void*)a.ws, 0, (int)W_END, 0x00020000);
  unsigned tag0 = 0, tag1 = 0;
  auto touch = [&](unsigned ph) {
    unsigned r = 0;
    if (m == 1u && lane < 8) {
      const unsigned p = min(ph + (lane >> 2), 15u);
      r = __builtin_nontemporal_load((const GAS unsigned*)((const GAS unsigned char*)(p < 8u ? a.k : a.v) +
                                     (((p & 7u) * 16u + g) * (3ull << 20) + min((lane & 3u) << 20, (3u << 20) - 4u))));
    }
    asm volatile("" :: "v"(r));
  };
  if (wave == 5) touch(0u);

  {
    const bool fj = m >= 12u;
    const unsigned id = fj ? 192u + 3u * g + m - 12u : m < 6u ? 6u * g + m : 6u * (((g + 2u) & 15u) + 15u) + m;
    u64 ep = 0;
    if (wave >= 5) ep = __hip_atomic_load((GAS u64*)a.ws, RLX_AGENT);
    if (m < 15u && wave != 5) {
      if (wave != 7) {
        const unsigned u5 = wave == 6 ? 4u : (unsigned)wave - 1u;
        const unsigned stride = fj ? kF * 2 : kHD * 2, row0 = (id & 1u) * 320u + u5 * 64u;
        const __amdgpu_buffer_rsrc_t rs = __builtin_amdgcn_make_buffer_rsrc((void*)(fj ? a.w1 : a.wq), 0, kD * stride, 0x00020000);
        const unsigned voff = (unsigned)(32 * ((lane >> 3) & 1) + 8 * g4) * stride + (unsigned)(lane & 7) * 16u;
        const unsigned base = ((id >> 1) - (fj ? 96u : 0u)) * 128u + row0 * stride;
        v4u av = *(const GAS v4u*)((const GAS unsigned char*)a.x + (lane & 3) * 1280 + (row0 + 32 * ((lane >> 2) & 1) + 8 * g4) * 2);
        wait_time(org0 + (GAP_IT * PACE >> 8) + u5 * STAG);
        v4u w[8];
#pragma unroll
        for (int j = 0; j < 8; ++j) {
          w[j] = __builtin_amdgcn_raw_buffer_load_b128(rs, voff, base + j * stride, AUX_NT);
        }
        if (lane & 8) av = v4u{0x3f803f80u, 0x3f803f80u, 0x3f803f80u, 0x3f803f80u};
        v4f acc[8];
#pragma unroll
        for (int d = 0; d < 4; ++d) {
          v4u b0, b1;
#pragma unroll
          for (int jj = 0; jj < 4; ++jj) {
            b0[jj] = __builtin_amdgcn_perm(w[2 * jj + 1][d], w[2 * jj][d], 0x05040100u);
            b1[jj] = __builtin_amdgcn_perm(w[2 * jj + 1][d], w[2 * jj][d], 0x07060302u);
          }
          acc[2 * d] = mfma(av, b0, v4f{0.f, 0.f, 0.f, 0.f});
          acc[2 * d + 1] = mfma(av, b1, v4f{0.f, 0.f, 0.f, 0.f});
        }
        if (g4 == ((lane >> 3) & 1)) {
          LAS unsigned char* p = L + L_PART + ((u5 * 2u + g4) * 64u + 8 * (lane & 7)) * 16u;
#pragma unroll
          for (int i = 0; i < 8; ++i) *(LAS v4f*)(p + i * 16) = acc[i];
        } else if (g4 == 2) {
          LAS unsigned char* p = L + L_CS + (u5 * 128u + n16 * 8) * 4u;
          *(LAS v4f*)p = v4f{acc[0][0], acc[1][0], acc[2][0], acc[3][0]};
          *(LAS v4f*)(p + 16) = v4f{acc[4][0], acc[5][0], acc[6][0], acc[7][0]};
        }
        wait_lds();
        ctl_add(L, C_PART, lane);
      } else {
        v4u xv[5];
#pragma unroll
        for (int j = 0; j < 5; ++j) xv[j] = *(const GAS v4u*)((const GAS unsigned char*)a.x + g4 * 1280 + (n16 + 16 * j) * 16);
        float sum = 0.f;
#pragma unroll
        for (int j = 0; j < 5; ++j)
#pragma unroll
          for (int d = 0; d < 4; ++d) sum += lo_f(xv[j][d]) + hi_f(xv[j][d]);
        const float mean = row16_sum(sum) * (1.f / kD);
        float sq = 0.f;
#pragma unroll
        for (int j = 0; j < 5; ++j)
#pragma unroll
          for (int d = 0; d < 4; ++d) { const float d0 = lo_f(xv[j][d]) - mean, d1 = hi_f(xv[j][d]) - mean; sq += d0 * d0; sq += d1 * d1; }
        const float rstd = 1.f / sqrtf(row16_sum(sq) * (1.f / kD) + 1e-5f);
        wait_ge<0>(ctl, C_PART, 5u, budget);
        v4f s = {0.f, 0.f, 0.f, 0.f};
        float cs = 0.f;
#pragma unroll
        for (int i = 0; i < 10; ++i) { s += *(const LAS v4f*)(L + L_PART + (i * 64 + lane) * 16); cs += *(const LAS float*)(L + L_CS + (i * 64 + lane) * 4); }
        float r[4];
#pragma unroll
        for (int b = 0; b < 4; ++b) r[b] = (s[b] - rdlane(mean, 16 * b) * cs) * rdlane(rstd, 16 * b);
        put(rw, lane * 16, W_QX + id * 1024u, pk_bf16(r[0], r[1]), pk_bf16(r[2], r[3]), (unsigned)ep + 1u, (unsigned)(ep >> 32) + 1u);
      }
    }
    if (wave >= 5) {
      tag0 = __builtin_amdgcn_readfirstlane((unsigned)ep) + 1u;
      tag1 = __builtin_amdgcn_readfirstlane((unsigned)(ep >> 32)) + 1u;
      asm volatile("" :: "v"(pf));
      const unsigned kind = wave - 5;
      if (kind < 2u) {
        wait_time(org0 + (GAP_IT * PACE >> 8) + (unsigned)(POLL_US * 100.0) + kind * 150u);
        v4u t[6];
        sweep<6, 8>(rw, lane * 16, W_QX + (kind * 16u + g) * 6144u, 1024u, tag0, tag1, t, budget);
#pragma unroll
        for (int j = 0; j < 6; ++j) {
          const unsigned u0 = t[j & 6][2 * (j & 1)], u1 = t[j | 1][2 * (j & 1)];
          const unsigned pk = pk_bf16(lo_f(u0) + lo_f(u1), hi_f(u0) + hi_f(u1));
          LAS unsigned char* q = L + L_QF + (kind * 4u + 2 * (j & 1)) * 384u + (64 * (j >> 1) + lane) * 2;
          st16(q, pk);
          st16(q + 384, pk >> 16);
        }
        wait_lds();
        ctl[C_Q + kind] = 1u;
      }
      if (kind == 1u && m < 15u) {
        const unsigned col = 96u * g + 32u * ks;
        const float bias = bf2f(((const GAS unsigned short*)a.b1)[col + (lane & 31)]);
        v4u t[1];
        sweep<1, 8>(rw, (lane >> 5) * 1024 + (lane & 31) * 16, W_QX + (96u + (col >> 6)) * 2048u + (col & 63u) * 16u, 0u, tag0, tag1, t, budget);
        const float r0 = fmaxf(swap32_sum(lo_f(t[0][0]), lo_f(t[0][2])) + bias, 0.f), r1 = fmaxf(swap32_sum(hi_f(t[0][0]), hi_f(t[0][2])) + bias, 0.f);
        const unsigned pk = pk_bf16(r0 * r0, r1 * r1);
        LAS unsigned char* o = L + L_OA + (lane >> 5) * 640 + 256 + (lane & 31) * 2;
        st16(o, pk);
        st16(o + 320, pk >> 16);
        wait_lds();
      }
      ctl[C_TAG] = tag0; ctl[C_TAG + 1] = tag1;
      wait_lds();
      ctl_add(L, C_PRO, lane);
    }
  }

  if (wave <= 4) {
    const int c = wave - 1;
    unsigned s = c;
    {
      const unsigned ka0 = n16 * 384 + ((g4 ^ (n16 >> 1)) << 4), ka1 = ka0 ^ 64u;
#pragma nounroll
      for (unsigned ph = 0; ph < 8u; ++ph) {
        v4u kf[4][12];
#pragma unroll
        for (int ii = 0; ii < 4; ++ii) {
          wait_ge<2>(ctl, C_FULL, ph * 16u + ii * 4u + c + 1u, budget);
          LAS unsigned char* p0 = L + s * SLOTB + ka0;
          LAS unsigned char* p1 = L + s * SLOTB + ka1;
#pragma unroll
          for (int t = 0; t < 6; ++t)
#pragma unroll
            for (int hf = 0; hf < 2; ++hf) kf[ii][hf * 6 + t] = *(const LAS v4u*)(((t & 1) ? p1 : p0) + hf * 6144 + (t >> 1) * 128);
          wait_lds();
          ctl[C_DONE + c] = ph * 4u + ii + 1u;
          s += 4; if (s >= NSLOT) s -= NSLOT;
        }
        if (ph < 3u) wait_ge<1>(ctl, ph < 2u ? C_Q + ph : C_PRO, ph < 2u ? 1u : 3u, budget);
        v4u qf[6];
#pragma unroll
        for (int t = 0; t < 6; ++t) qf[t] = *(const LAS v4u*)(L + L_QF + ((ph & 1u) * 4u + (ph >> 1)) * 384u + t * 64 + g4 * 16);
#pragma unroll
        for (int ii = 0; ii < 4; ii += 2) {
          v4f d[4];
#pragma unroll
          for (int i = 0; i < 4; ++i) d[i] = v4f{0.f, 0.f, 0.f, 0.f};
#pragma unroll
          for (int t = 0; t < 6; ++t)
#pragma unroll
            for (int i = 0; i < 4; ++i) d[i] = mfma(qf[t], kf[ii + (i >> 1)][(i & 1) * 6 + t], d[i]);
          const float sc = sel(g4 & 2, sel(g4 & 1, d[3][0], d[2][0]), sel(g4 & 1, d[1][0], d[0][0]));
          *(LAS float*)(L + L_SC + ph * 2048u + c * 128 + (ii + (lane >> 5)) * 512 + (lane & 31) * 4) = sc;
        }
        wait_lds();
        ctl[C_S + c] = ph + 1u;
      }
    }
    tag0 = ctl[C_TAG]; tag1 = ctl[C_TAG + 1];
    {
      const int q = n16 >> 2, p = lane & 3;
      unsigned va[2];
#pragma unroll
      for (int t = 0; t < 2; ++t) {
        const int row = 8 * g4 + (q & 1) + 4 * (q >> 1) + 2 * t;
        va[t] = row * 384 + ((((p >> 1)) ^ ((row >> 1) & 7)) << 4) + 8 * (p & 1);
      }
#pragma nounroll
      for (unsigned ph = 0; ph < 8u; ++ph) {
        wait_ge<1>(ctl, C_P, ph + 1u, budget);
        v4f acc[12];
#pragma unroll
        for (int i = 0; i < 12; ++i) acc[i] = v4f{0.f, 0.f, 0.f, 0.f};
#pragma nounroll
        for (unsigned ii = 0; ii < 4u; ++ii) {
          const unsigned sub = ii * 4u + c, it = 128u + ph * 16u + sub;
          const v4u pa = *(const LAS v4u*)(L + L_SC + ph * 2048u + (lane & 1) * 1024 + sub * 64u + g4 * 16);
          wait_ge<1>(ctl, C_FULL, it + 1u, budget);
          const unsigned b0 = s * SLOTB + va[0], b1 = s * SLOTB + va[1];
          v4u vf[12];
#pragma unroll
          for (int t = 0; t < 12; ++t) vf[t] = tr_read2(L + (b0 ^ ((t & 3) << 5)) + (t >> 2) * 128, L + (b1 ^ ((t & 3) << 5)) + (t >> 2) * 128);
          __builtin_amdgcn_sched_barrier(0);
#pragma unroll
          for (int t = 0; t < 12; ++t) acc[t] = mfma(pa, vf[t], acc[t]);
          wait_lds();
          ctl[C_DONE + c] = (it >> 2) + 1u;
          s += 4; if (s >= NSLOT) s -= NSLOT;
        }
        v4f o;
#pragma unroll
        for (int i = 0; i < 4; ++i)
          o[i] = sel(g4 & 2, acc[8 + i][0], sel(g4 & 1, acc[4 + i][0], acc[i][0])) + sel(g4 & 2, acc[8 + i][1], sel(g4 & 1, acc[4 + i][1], acc[i][1]));
        LAS unsigned char* ob = L + L_OPB + (ph & 1u) * 2304u + (lane < 48 ? lane : lane - 16) * 16;
        if (c < 3) {
          if (ph >= 2u) wait_ge<1>(ctl, C_OFREE, ph - 1u, budget);
          *(LAS v4f*)(ob + c * 768) = o;
          wait_lds();
          ctl[C_ODONE + c] = ph + 1u;
        } else {
          wait_ge<0>(ctl, C_ODONE + 0, ph + 1u, budget); wait_ge<0>(ctl, C_ODONE + 1, ph + 1u, budget); wait_ge<0>(ctl, C_ODONE + 2, ph + 1u, budget);
          o = ((*(const LAS v4f*)ob + *(const LAS v4f*)(ob + 768)) + *(const LAS v4f*)(ob + 1536)) + o;
          v2u pk = {pk_bf16(o[0], o[1]), pk_bf16(o[2], o[3])};
          if (lane < 48) {
            if (ph < 7u) *(LAS v2u*)(L + L_ST + ph * 384u + lane * 8) = pk;
            else put(rw, lane * 16, W_H1 + wg * 768u, pk[0], pk[1], tag0, tag1);
          }
          wait_lds();
          ctl[C_OFREE] = ph + 1u;
        }
      }
    }
    if (m < 15u) {
      wait_ge<1>(ctl, C_G, 7u, budget);
      const int q = n16 >> 2;
      const int bo = (8 * g4 + q) * 256 + ((c * 64) ^ ((q | (g4 & 1) << 2) << 5)) + (lane & 3) * 8;
      LAS unsigned char* ba = L + L_TAIL + bo;
      LAS unsigned char* bb = L + L_TAIL + (bo ^ 32);
      LAS unsigned char* aa = L + L_OA + (lane & 3) * 320 + g4 * 16;
      const v4u a0 = *(const LAS v4u*)aa, a1 = *(const LAS v4u*)(aa + 64), a4 = *(const LAS v4u*)(aa + 256);
      wait_ge<0>(ctl, C_WO, 1u, budget);
      v4u f0[5], f1[5];
#pragma unroll
      for (int t = 0; t < 5; ++t) { f0[t] = tr_read2(ba + t * 8192, ba + t * 8192 + 1024); f1[t] = tr_read2(bb + t * 8192, bb + t * 8192 + 1024); }
      v4f y0 = mfma(a0, f0[0], v4f{0.f, 0.f, 0.f, 0.f}), y1 = mfma(a0, f1[0], v4f{0.f, 0.f, 0.f, 0.f});
      y0 = mfma(a1, f0[1], y0); y1 = mfma(a1, f1[1], y1);
      y0 = mfma(a4, f0[4], y0); y1 = mfma(a4, f1[4], y1);
      asm volatile("" : "+v"(y0), "+v"(y1), "+v"(f0[2]), "+v"(f0[3]), "+v"(f1[2]), "+v"(f1[3]) :: "memory");
      wait_ge<0>(ctl, C_G, 8u, budget);
      const v4u a2 = *(const LAS v4u*)(aa + 128), a3 = *(const LAS v4u*)(aa + 192);
      y0 = mfma(a2, f0[2], y0); y1 = mfma(a2, f1[2], y1);
      y0 = mfma(a3, f0[3], y0); y1 = mfma(a3, f1[3], y1);
      const float z0 = sel(g4 & 2, sel(g4 & 1, y1[2], y1[0]), sel(g4 & 1, y0[2], y0[0]));
      const float z1 = sel(g4 & 2, sel(g4 & 1, y1[3], y1[1]), sel(g4 & 1, y0[3], y0[1]));
      put(rw, (lane >> 3) * 6144 + (lane & 7) * 16, W_H2 + (cb * 32u + c * 8u) * 6144u + (g * 3u + ks) * 128u, asu(z0), asu(z1), tag0, tag1);
    }
    return;
  }

  if (wave == 7) {
    {
      float tg = 0.f, e = 0.f;
      constexpr float rate = FB_RAMP / (1.0 + FB_RAMP) * 100.0;
#pragma nounroll
      for (unsigned k = 0; k < FB_N; ++k) {
        const unsigned tw = org0 + (unsigned)(FB_0 * 100.0) + k * (unsigned)(FB_P * 100.0) + __builtin_amdgcn_readfirstlane((unsigned)e);
        wait_time(tw);
        const float late = (float)(int)ctl[C_LATE] - e;
        if ((wg & 3u) == 0u && lane == 0 && late > (float)(FB_THR * 100.0)) put(rw, 0, W_FB + k * 1024u + wg * 4u, asu(late), 0u, tag0, tag1);
        touch(k + 1u);
        wait_time(tw + (unsigned)(FB_D * 100.0) + (wg & 31u) * 2u);
        const v4u t = __builtin_amdgcn_raw_buffer_load_b128(rw, lane * 16, W_FB + k * 1024u, AUX_DEV);
        const float v = t[1] == tag0 && t[3] == tag1 ? fminf(asf(t[0]), 1000.f) : 0.f;
        float x = row16_sum(v), y = row16_max(-v);
        x = (rdlane(x, 0) + rdlane(x, 16)) + (rdlane(x, 32) + rdlane(x, 48));
        y = e - fmaxf(fmaxf(rdlane(y, 0), rdlane(y, 16)), fmaxf(rdlane(y, 32), rdlane(y, 48)));
        const float e1 = fmaxf(fminf(tg, e + rate * (float)FB_D), y);
        tg = fmaxf(tg, e + x * (FB_G / 64.f));
        e = fminf(tg, e1 + rate * (float)(FB_P - FB_D));
        *(volatile LAS v2u*)(L + L_CTL + C_SH * 4) = v2u{(unsigned)(tg * 256.f), (unsigned)(y * 256.f)};
        touch(k + 1u);
      }
    }
    if (m < 15u) {
#pragma nounroll
      for (unsigned q = 256u - NSLOT; q < 260u - NSLOT; ++q) wait_ge<8>(ctl, C_DONE + (q & 3u), (q >> 2) + 1u, budget);
      wait_ge<1>(ctl, C_FULL, TAIL_IT, budget);
      const __amdgpu_buffer_rsrc_t ro = __builtin_amdgcn_make_buffer_rsrc((void*)a.wo, 0, kHD * kD * 2, 0x00020000);
      const __amdgpu_buffer_rsrc_t r2 = __builtin_amdgcn_make_buffer_rsrc((void*)a.w2, 0, kF * kD * 2, 0x00020000);
      const int ch = (n16 ^ (2 * g4)) * 16;
#pragma nounroll
      for (unsigned p = 0; p < 32u; p += 4u) {
        const unsigned so = (g * 192u + ks * 64u + (p >> 4) * 3072u + (p & 15u)) * 1280u + cb * 256u;
#pragma unroll
        for (int j = 0; j < 4; ++j)
          __builtin_amdgcn_raw_ptr_buffer_load_lds(ro, (LAS void*)(L + L_TAIL + (p + j) * 1024), 16, g4 * 20480 + (ch ^ ((j & 2) << 6)), so + j * 1280, 0, AUX_NT);
      }
#pragma unroll
      for (int j = 0; j < 8; ++j)
        __builtin_amdgcn_raw_ptr_buffer_load_lds(r2, (LAS void*)(L + L_TAIL + (32 + j) * 1024), 16, g4 * 1280 + (ch ^ ((j & 2) << 6)), (g * 96u + ks * 32u + 4u * j) * 1280u + cb * 256u, 0, AUX_NT);
      wait_vm<0>();
      ctl[C_WO] = 1u;
    }
    return;
  }
  if (wave == 6) {
    if (m < 15u) {
      wait_ge<8>(ctl, C_FULL, ML_IT, budget);
      {
        v4u t[2];
        sweep<2, 16>(rw, n16 * 128 + g4 * 16, W_ML + g * 2048u, 64u, tag0, tag1, t, budget);
#pragma unroll
        for (int h = 0; h < 2; ++h) {
          const float mj = asf(t[h][0]), lj = asf(t[h][2]);
          const float wj = __builtin_amdgcn_exp2f((mj - row16_max(mj)) * kC);
          *(LAS float*)(L + L_WT + h * 256 + lane * 4) = wj / row16_sum(wj * lj);
        }
      }
      cbar();
      wait_ge<2>(ctl, C_FULL, H0_IT, budget);
      if (lane < 56) {
        v4u t[16];
#pragma unroll
        for (int i = 0; i < 16; ++i) t[i] = __builtin_amdgcn_raw_buffer_load_b128(rw, lane * 16, W_H0 + (g * 16u + i) * 3072u + ks * 1024u, AUX_DEV);
        float v[8];
#pragma unroll
        for (int i = 0; i < 8; ++i) v[i] = 0.f;
#pragma unroll
        for (int i = 0; i < 16; i += 4) {
          const v4f wt = *(const LAS v4f*)(L + L_WT + (lane >> 3) * 64 + i * 4);
#pragma unroll
          for (int k = 0; k < 4; ++k)
#pragma unroll
            for (int d = 0; d < 4; ++d) { v[2 * d] += lo_f(t[i + k][d]) * wt[k]; v[2 * d + 1] += hi_f(t[i + k][d]) * wt[k]; }
        }
        *(LAS v4u*)(L + L_OA + g4 * 320 + n16 * 16) = v4u{pk_bf16(v[0], v[1]), pk_bf16(v[2], v[3]), pk_bf16(v[4], v[5]), pk_bf16(v[6], v[7])};
      }
      wait_lds();
      ctl[C_G] = 7u;
      const v4f wt = *(const LAS v4f*)(L + L_WT + 7 * 64 + g4 * 16);
      wait_ge<1>(ctl, C_FULL, 256u, budget);
      v4u t[4];
      __builtin_amdgcn_s_sleep(DLY1);
      sweep<4, 0>(rw, g4 * 3072 + n16 * 16, W_H1 + g * 12288u + ks * 256u, 768u, tag0, tag1, t, budget);
      float v[4];
#pragma unroll
      for (int i = 0; i < 2; ++i) {
        v[2 * i] = (lo_f(t[0][2 * i]) * wt[0] + lo_f(t[1][2 * i]) * wt[1]) + (lo_f(t[2][2 * i]) * wt[2] + lo_f(t[3][2 * i]) * wt[3]);
        v[2 * i + 1] = (hi_f(t[0][2 * i]) * wt[0] + hi_f(t[1][2 * i]) * wt[1]) + (hi_f(t[2][2 * i]) * wt[2] + hi_f(t[3][2 * i]) * wt[3]);
      }
      const float o = swap16_sum(swap32_sum(v[0], v[2]), swap32_sum(v[1], v[3]));
      st16(L + L_OA + 3 * 320 + 128 + n16 * 8 + g4 * 2, pk_bf16(o, 0.f));
      wait_lds();
      ctl[C_G] = 8u;
    }
    return;
  }
  {
    const unsigned j = g * 10u + (m < 10u ? m : 0u);
    const unsigned col = (j >> 3) * 32u + (j & 4u) * 4u + (j & 1u) * 8u + (lane & 7);
    const unsigned oidx = ((j & 2u) + (lane >> 5)) * kD + col;
    const float resid = bf2f(((const GAS unsigned short*)a.x)[oidx]) + bf2f(((const GAS unsigned short*)a.b2)[col]);
    wait_ge<1>(ctl, C_PRO, 3u, budget);
    float mxs = 0.f, ls = 0.f;
#pragma nounroll
    for (unsigned ph = 0; ph < 8u; ++ph) {
#pragma nounroll
      for (int c = 0; c < 4; ++c) wait_ge<4>(ctl, C_S + c, ph + 1u, budget);
      LAS unsigned char* sp = L + L_SC + ph * 2048u;
      const v4f s0 = *(const LAS v4f*)(sp + lane * 32), s1 = *(const LAS v4f*)(sp + lane * 32 + 16);
      float mx = fmaxf(fmaxf(fmaxf(s0[0], s0[1]), fmaxf(s0[2], s0[3])), fmaxf(fmaxf(s1[0], s1[1]), fmaxf(s1[2], s1[3])));
      mx = row16_max(mx);
      mx = fmaxf(fmaxf(rdlane(mx, 0), rdlane(mx, 16)), fmaxf(rdlane(mx, 32), rdlane(mx, 48)));
      float p[8];
#pragma unroll
      for (int i = 0; i < 4; ++i) { p[i] = __builtin_amdgcn_exp2f((s0[i] - mx) * kC); p[4 + i] = __builtin_amdgcn_exp2f((s1[i] - mx) * kC); }
      float l = ((p[0] + p[1]) + (p[2] + p[3])) + ((p[4] + p[5]) + (p[6] + p[7]));
      l = row16_sum(l);
      l = (rdlane(l, 0) + rdlane(l, 16)) + (rdlane(l, 32) + rdlane(l, 48));
      v4u hi, lo;
#pragma unroll
      for (int d = 0; d < 4; ++d) {
        const int i = (d == 1) ? 4 : (d == 2) ? 2 : 2 * d;
        hi[d] = pk_bf16(p[i], p[i + 1]);
        lo[d] = pk_bf16(p[i] - lo_f(hi[d]), p[i + 1] - hi_f(hi[d]));
      }
      cbar();
      *(LAS v4u*)(sp + lane * 16) = hi;
      *(LAS v4u*)(sp + 1024 + lane * 16) = lo;
      if ((unsigned)lane == ph) { mxs = mx; ls = l; }
      wait_lds();
      ctl[C_P] = ph + 1u;
    }
    wait_ge<8>(ctl, C_FULL, PUB_IT, budget);
    wait_ge<2>(ctl, C_OFREE, 7u, budget);
    if (lane < 56) {
#pragma unroll
      for (int i = 0; i < 3; ++i)
        __builtin_amdgcn_raw_buffer_store_b128(*(const LAS v4u*)(L + L_ST + (lane >> 3) * 384 + i * 128 + (lane & 7) * 16), rw, lane * 16, W_H0 + wg * 3072u + i * 1024u, AUX_DEV);
    }
    wait_vm<0>();
    if (lane < 8) put(rw, lane * 16, W_ML + wg * 128u, asu(mxs), asu(ls), tag0, tag1);
    if (m < 10u) {
      wait_ge<8>(ctl, C_FULL, 250u, budget);
      wait_ge<1>(ctl, C_G, 8u, budget);
      v4u t[6];
      __builtin_amdgcn_s_sleep(DLY2);
      sweep<6, 0>(rw, lane * 16, W_H2 + j * 6144u, 1024u, tag0, tag1, t, budget);
      const float y0 = ((asf(t[0][0]) + asf(t[1][0])) + (asf(t[2][0]) + asf(t[3][0]))) + (asf(t[4][0]) + asf(t[5][0]));
      const float y1 = ((asf(t[0][2]) + asf(t[1][2])) + (asf(t[2][2]) + asf(t[3][2]))) + (asf(t[4][2]) + asf(t[5][2]));
      float y = swap32_sum(y0, y1);
      y = swap16_sum(y, y);
      y += dpp<0x128>(y);
      if ((lane & 24) == 0) ((GAS unsigned short*)a.out)[oidx] = (unsigned short)pk_bf16(y + resid, 0.f);
      if (wg == 0u && lane == 0) __hip_atomic_store((GAS u64*)a.ws, ((u64)tag1 << 32) | tag0, RLX_AGENT);
    }
  }
}

}

extern "C" void kernel_launch(void* const* d_in, const int* in_sizes, int n_in,
                              void* d_out, int out_size, void* d_ws, size_t ws_size,
                              hipStream_t stream) {
  (void)in_sizes; (void)n_in; (void)out_size; (void)ws_size;
  Args a;
  a.x = (const unsigned short*)d_in[0];
  a.k = (const unsigned short*)d_in[1];
  a.v = (const unsigned short*)d_in[2];
  a.wq = (const unsigned short*)d_in[3];
  a.wo = (const unsigned short*)d_in[4];
  a.w1 = (const unsigned short*)d_in[6];
  a.b1 = (const unsigned short*)d_in[7];
  a.w2 = (const unsigned short*)d_in[8];
  a.b2 = (const unsigned short*)d_in[9];
  a.out = (unsigned short*)d_out;
  a.ws = (unsigned char*)d_ws;
  (void)hipFuncSetAttribute((const void*)fused_decode, hipFuncAttributeMaxDynamicSharedMemorySize, L_BYTES);
  fused_decode<<<256, 512, L_BYTES, stream>>>(a);
}
```
